```python
import torch

def _softmax(x, axis=-1):
    return torch.softmax(x, dim=axis)

def _gelu(x):
    return torch.nn.functional.gelu(x, approximate='tanh')

def _silu(x):
    return torch.nn.functional.silu(x)

def _sigmoid(x):
    return torch.sigmoid(x)

def _layernorm(x, axis=-1, eps=1e-05):
    m = x.mean(dim=axis, keepdim=True)
    v = x.var(dim=axis, keepdim=True, unbiased=False)
    return (x - m) / torch.sqrt(v + eps)

def _scatter_add(tgt, idx, src, axis=0):
    return tgt.clone().index_add_(axis, idx, src)

def _segment_sum(src, seg, n):
    out = torch.zeros((n,) + src.shape[1:], dtype=src.dtype)
    return out.index_add_(0, seg, src)

def _pad(x, pw):
    flat = tuple((v for pair in reversed(pw) for v in pair))
    return torch.nn.functional.pad(x, flat)
kernel_name = 'standalone__window_attn_twins__vision'

def setup_inputs():

    _w = lambda a: (lambda t: t.to(torch.bfloat16) if t.is_floating_point() else t)(torch.as_tensor(a))
    H = W = 224 // 7 * 7
    C = 96
    return {'x': _w((lambda *s: torch.randn(*s, dtype=torch.bfloat16) * 0.8)(1, H, W, C)), 'wq': _w((lambda *s: torch.randn(*s, dtype=torch.bfloat16) * 0.8)(C, C)), 'wkv': _w((lambda *s: torch.randn(*s, dtype=torch.bfloat16) * 0.8)(C, 2 * C)), 'wo': _w((lambda *s: torch.randn(*s, dtype=torch.bfloat16) * 0.05)(C, C))}

def reference(x, wq, wkv, wo):
    B, H, W, C = x.shape
    win = 7
    nh, nw = (H // win, W // win)
    xs = x.reshape(B, nh, win, nw, win, C).mean(axis=(2, 4)).reshape(B, nh * nw, C)
    q = x.reshape(B, H * W, C) @ wq
    kv = xs @ wkv
    k, v = (kv[..., :C].clone(), kv[..., C:].clone())
    scale = C ** (-0.5)
    p = _softmax(torch.einsum('bqc,bkc->bqk', q, k) * scale, axis=-1)
    o = torch.einsum('bqk,bkc->bqc', p, v) @ wo
    return o.reshape(B, H, W, C)
```

```cpp
#include <hip/hip_runtime.h>
#include <hip/hip_bf16.h>
#include <stdint.h>

typedef float f32x16 __attribute__((ext_vector_type(16)));
typedef float f32x2 __attribute__((ext_vector_type(2)));
typedef __bf16 bf16x8 __attribute__((ext_vector_type(8)));
typedef __bf16 bf16x2 __attribute__((ext_vector_type(2)));
typedef unsigned u32x4 __attribute__((ext_vector_type(4)));
#define DI __device__ __forceinline__
#define MFMA(a, b, c) __builtin_amdgcn_mfma_f32_32x32x16_bf16((a), (b), (c), 0, 0, 0)

constexpr int C = 96, HW = 224, NTOK = HW * HW, NKEY = 1024;
DI float bf2f(unsigned short u) { return __uint_as_float((unsigned)u << 16); }
DI unsigned short f2bf(float f) { __bf16 b = (__bf16)f; return __builtin_bit_cast(unsigned short, b); }
DI unsigned cvtpk(float lo, float hi) { f32x2 v = {lo, hi}; bf16x2 b = __builtin_convertvector(v, bf16x2); return __builtin_bit_cast(unsigned, b); }

__global__ __launch_bounds__(192) void prep(const unsigned short* x, const unsigned short* wq, const unsigned short* wkv, const unsigned short* wo, unsigned short* kv) {
  __shared__ float xs[C], kvs[2 * C];
  const int w = blockIdx.x, wi = w >> 5, wj = w & 31, t = threadIdx.x;
  if (t < C) { float s = 0; for (int a = 0; a < 7; ++a) for (int b = 0; b < 7; ++b) s += bf2f(x[((wi * 7 + a) * HW + wj * 7 + b) * C + t]); xs[t] = bf2f(f2bf(s / 49.f)); }
  __syncthreads();
  { float s = 0; for (int c = 0; c < C; ++c) s += xs[c] * bf2f(wkv[c * 2 * C + t]); kvs[t] = bf2f(f2bf(s)); }
  __syncthreads();
  const int tile = w >> 5, key = w & 31;
  unsigned short* base = kv + tile * 6144;
  if (t < C) {
    float s = 0; for (int c = 0; c < C; ++c) s += kvs[c] * bf2f(wq[t * C + c]);
    s *= 0.10206207261596575f * 1.4426950408889634f;
    const int f = t >> 4, h = (t >> 3) & 1, j = t & 7;
    base[f * 512 + (h * 32 + key) * 8 + j] = f2bf(s);
  } else { const int d = t - C;
    float s = 0; for (int c = 0; c < C; ++c) s += kvs[C + c] * bf2f(wo[c * C + d]);
    const int ch = d >> 3, db = ch >> 2, tt = (ch >> 1) & 1, hh = ch & 1, e = d & 7;
    const int i = (e & 3) + 4 * hh + 8 * ((e >> 2) + 2 * tt);
    const int ks = key >> 4, h = (key >> 2) & 1, j = ((key >> 3) & 1) * 4 + (key & 3);
    base[3072 + (db * 2 + ks) * 512 + (h * 32 + i) * 8 + j] = f2bf(s);
  }
}

__global__ __launch_bounds__(512) void attn(const unsigned short* x, const unsigned short* kv, unsigned short* out) {
  const int lane = threadIdx.x & 63, wid = threadIdx.x >> 6, r = lane & 31, h = lane >> 5;
  const int row = (blockIdx.x * 8 + wid) * 32 + r;
  bf16x8 q[6];
#pragma unroll
  for (int f = 0; f < 6; ++f) q[f] = *(const bf16x8*)(x + row * C + f * 16 + h * 8);
  f32x16 o[3] = {};
  float l = 0;
  for (int t = 0; t < 32; ++t) {
    const bf16x8* p = (const bf16x8*)(kv + t * 6144) + lane;
    f32x16 s = {};
#pragma unroll
    for (int f = 0; f < 6; ++f) s = MFMA(p[f * 64], q[f], s);
#pragma unroll
    for (int i = 0; i < 16; ++i) { s[i] = __builtin_amdgcn_exp2f(s[i]); l += s[i]; }
#pragma unroll
    for (int ks = 0; ks < 2; ++ks) {
      u32x4 pk = {cvtpk(s[8 * ks], s[8 * ks + 1]), cvtpk(s[8 * ks + 2], s[8 * ks + 3]), cvtpk(s[8 * ks + 4], s[8 * ks + 5]), cvtpk(s[8 * ks + 6], s[8 * ks + 7])};
      bf16x8 pb = __builtin_bit_cast(bf16x8, pk);
#pragma unroll
      for (int db = 0; db < 3; ++db) o[db] = MFMA(p[(6 + db * 2 + ks) * 64], pb, o[db]);
    }
  }
  l += __shfl_xor(l, 32);
  const float rl = 1.f / l;
#pragma unroll
  for (int db = 0; db < 3; ++db)
#pragma unroll
    for (int tt = 0; tt < 2; ++tt) {
      u32x4 w = {cvtpk(o[db][8 * tt] * rl, o[db][8 * tt + 1] * rl), cvtpk(o[db][8 * tt + 2] * rl, o[db][8 * tt + 3] * rl), cvtpk(o[db][8 * tt + 4] * rl, o[db][8 * tt + 5] * rl), cvtpk(o[db][8 * tt + 6] * rl, o[db][8 * tt + 7] * rl)};
      *(u32x4*)(out + row * C + (4 * db + 2 * tt + h) * 8) = w;
    }
}

extern "C" void kernel_launch(void* const* d_in, const int* in_sizes, int n_in, void* d_out, int out_size, void* d_ws, size_t ws_size, hipStream_t stream) {
  const unsigned short* x = (const unsigned short*)d_in[0];
  prep<<<NKEY, 192, 0, stream>>>(x, (const unsigned short*)d_in[1], (const unsigned short*)d_in[2], (const unsigned short*)d_in[3], (unsigned short*)d_ws);
  attn<<<NTOK / 256, 512, 0, stream>>>(x, (const unsigned short*)d_ws, (unsigned short*)d_out);
}
```

```cpp
#include <hip/hip_runtime.h>
#include <hip/hip_bf16.h>
#include <stdint.h>

typedef float f32x4 __attribute__((ext_vector_type(4)));
typedef float f32x2 __attribute__((ext_vector_type(2)));
typedef __bf16 bf16x8 __attribute__((ext_vector_type(8)));
typedef __bf16 bf16x2 __attribute__((ext_vector_type(2)));
typedef short s16x4 __attribute__((ext_vector_type(4)));
typedef unsigned u32x4 __attribute__((ext_vector_type(4)));
typedef unsigned u32x2 __attribute__((ext_vector_type(2)));
typedef unsigned long long u64;
#define LAS __attribute__((address_space(3)))
#define GAS __attribute__((address_space(1)))
#define DI __device__ __forceinline__
#define MFMA(a, b, c) __builtin_amdgcn_mfma_f32_16x16x32_bf16((a), (b), (c), 0, 0, 0)

constexpr int NREP = 8;
constexpr unsigned WS_FLAGS = 196608, WS_WFL = 213504, WS_W = 217600;
constexpr unsigned L_WQ = 24576, L_WKV = 43008, L_WF = 98304, L_X = 116736, L_ZERO = L_X + 37632, L_TOTAL = L_ZERO + 256;
typedef unsigned u32x8 __attribute__((ext_vector_type(8)));
typedef unsigned u32x16 __attribute__((ext_vector_type(16)));

DI unsigned cvtpk(float lo, float hi) { f32x2 v = {lo, hi}; bf16x2 b = __builtin_convertvector(v, bf16x2); return __builtin_bit_cast(unsigned, b); }
DI bf16x8 pack8(const f32x4& a, const f32x4& b) { u32x4 w = {cvtpk(a[0], a[1]), cvtpk(a[2], a[3]), cvtpk(b[0], b[1]), cvtpk(b[2], b[3])}; return __builtin_bit_cast(bf16x8, w); }
DI unsigned token_off(int T) {
  const int n = (T * 1338) >> 16, tau = T - 49 * n, r = (tau * 37) >> 8;
  return (unsigned)(r * 43008 + (7 * n + tau - 7 * r) * 192);
}

DI bf16x8 ldsv(unsigned a) { return *(LAS bf16x8*)(size_t)a; }
DI bf16x8 tr8(unsigned a0, unsigned a1) {
  s16x4 lo = __builtin_amdgcn_ds_read_tr16_b64_v4i16((LAS s16x4*)(size_t)a0), hi = __builtin_amdgcn_ds_read_tr16_b64_v4i16((LAS s16x4*)(size_t)a1);
  return __builtin_bit_cast(bf16x8, __builtin_shufflevector(lo, hi, 0, 1, 2, 3, 4, 5, 6, 7));
}
DI void slow_block(unsigned qa, unsigned sc, unsigned kbase, unsigned vbase, const char* ws, char* o, bool valid) {
  const int lane = __builtin_amdgcn_mbcnt_hi(~0u, __builtin_amdgcn_mbcnt_lo(~0u, 0));
  const __amdgpu_buffer_rsrc_t rs = __builtin_amdgcn_make_buffer_rsrc((void*)ws, 0, WS_FLAGS, 0x00020000);
  bf16x8 q[3];
#pragma unroll
  for (int f = 0; f < 3; ++f) q[f] = ldsv(qa + 1024 * f);
  f32x4 acc[6] = {};
  float m = -INFINITY, l = 0.f;
  for (int t = 0; t < 32; ++t) {
#pragma unroll
    for (int k = 0; k < 6; ++k) *(LAS u32x4*)(size_t)(sc + k * 1024 + lane * 16) = __builtin_amdgcn_raw_buffer_load_b128(rs, t * 6144 + k * 1024 + lane * 16, 0, 16);
    asm volatile("s_waitcnt lgkmcnt(0)" ::: "memory");
    f32x4 s[2] = {};
#pragma unroll
    for (int k = 0; k < 6; ++k) s[k / 3] = MFMA(ldsv(sc + kbase + (k / 3) * 3072 + (k % 3) * 64), q[k % 3], s[k / 3]);
    float mx = fmaxf(fmaxf(fmaxf(s[0][0], s[0][1]), fmaxf(s[0][2], s[0][3])), fmaxf(fmaxf(s[1][0], s[1][1]), fmaxf(s[1][2], s[1][3])));
    mx = fmaxf(mx, __shfl_xor(mx, 16)); mx = fmaxf(mx, __shfl_xor(mx, 32));
    const float mn = fmaxf(m, mx), mref = mn == -INFINITY ? 0.f : mn, al = __builtin_amdgcn_exp2f(m - mref);
    m = mn; l *= al;
#pragma unroll
    for (int i = 0; i < 8; ++i) { const float p = __builtin_amdgcn_exp2f(s[i >> 2][i & 3] - mref); s[i >> 2][i & 3] = p; l += p; }
    const bf16x8 pb = pack8(s[0], s[1]);
#pragma unroll
    for (int k = 0; k < 6; ++k) { const unsigned a = sc + (vbase ^ ((k & 1) * 32)) + (k >> 1) * 64; acc[k] = MFMA(tr8(a, a + 3072), pb, acc[k] * al); }
    asm volatile("s_waitcnt lgkmcnt(0)" ::: "memory");
  }
  l += __shfl_xor(l, 16); l += __shfl_xor(l, 32);
  const float rl = 1.f / (49.f * l);
  f32x4 r[6] = {};
#pragma unroll
  for (int s = 0; s < 3; ++s) {
    const bf16x8 ob = pack8(acc[2 * s] * rl, acc[2 * s + 1] * rl);
#pragma unroll
    for (int B = 0; B < 6; ++B) r[B] = MFMA(ldsv(L_WF + (3 * B + s) * 1024 + lane * 16), ob, r[B]);
  }
  if (valid) {
#pragma unroll
    for (int p = 0; p < 3; ++p) *(bf16x8*)(o + 64 * p) = pack8(r[2 * p], r[2 * p + 1]);
  }
}

__global__ __launch_bounds__(256, 1) __attribute__((aligned(4096))) void twins_attn(const char*, const char*, const char*, const char*, char*, char*) {
  __shared__ __attribute__((aligned(16))) char lds[L_TOTAL];
  u32x8 ka; u32x4 kb;
  asm volatile("s_load_dwordx8 s[12:19], %2, 0\n\ts_load_dwordx4 s[20:23], %2, 32" : "=&{s[12:19]}"(ka), "=&{s[20:23]}"(kb) : "s"(__builtin_amdgcn_kernarg_segment_ptr()) : "memory");
  int lane = threadIdx.x & 63;
  const int wid = __builtin_amdgcn_readfirstlane(threadIdx.x >> 6), blk = blockIdx.x;
  const int r = blk >> 3, tile = 4 * (blk & 7) + (r >> 3), p = r & 7, half = wid >> 1, par = wid & 1;
  {
    unsigned o = lane < 32 ? lane * 128 : 4096 + ((blk >> 3) * 10 + min(lane - 32, 9)) * 128;
    asm volatile("s_getpc_b64 s[36:37]\n\ts_and_b32 s36, s36, 0xfffff000\n\tv_min_u32 %0, twins_end-twins_a+4096, %0\n\tglobal_load_dword a246, %0, s[36:37]" : "+v"(o) :: "memory", "scc", "s36", "s37", "a246");
  }
  unsigned xa[3], xb2[3];
#pragma unroll
  for (int j = 0; j < 3; ++j) { xa[j] = (lane * 16) ^ (((lane * 5462 + (j * 64 * 5462 + ((wid * 49 + 4) << 16))) >> 14) & 48); xb2[j] = xa[j] + 41664; }
  unsigned xd = L_X + wid * 9408, tk = (tile * 7 * 224 + p * 28) * 192, tw = tk + wid * 1344;
  asm volatile("" : "+v"(xa[0]), "+v"(xa[1]), "+v"(xa[2]), "+v"(xb2[0]), "+v"(xb2[1]), "+v"(xb2[2]));
  unsigned m0save, junk;
  asm volatile("s_mov_b32 %0, m0\n\ts_waitcnt lgkmcnt(0)\n\ts_load_dword s75, s[22:23], 213504" : "=&s"(m0save), "+{s[12:19]}"(ka), "+{s[20:23]}"(kb), "+s"(xd), "+s"(tk), "+s"(tw), "=&{s75}"(junk) : "s"((unsigned)(size_t)(LAS char*)lds) : "memory");
  auto ptr = [](unsigned lo, unsigned hi) { return (char*)(GAS char*)(((u64)hi << 32) | lo); };
  const char *x = ptr(ka[0], ka[1]), *wq = ptr(ka[2], ka[3]), *wkv = ptr(ka[4], ka[5]), *wo = ptr(ka[6], ka[7]);
  char *out = ptr(kb[0], kb[1]), *ws = ptr(kb[2], kb[3]);
  const size_t tok0 = tk;
  unsigned xt;
  asm volatile(
               "s_mov_b32 m0, %8\n\t"
               "s_nop 0\n\t"
               "global_load_lds_dwordx4 %1, %7 nt\n\t"
               "s_lshl_b64 vcc, -1, 20\n\t"
               "s_add_u32 m0, %8, 1024\n\t"
               "v_cndmask_b32 %0, %2, %5, vcc\n\t"
               "v_add_u32 %0, 1024, %0\n\t"
               "global_load_lds_dwordx4 %0, %7 nt\n\t"
               "s_lshl_b64 vcc, -1, 40\n\t"
               "s_add_u32 m0, %8, 2048\n\t"
               "v_cndmask_b32 %0, %3, %6, vcc\n\t"
               "v_add_u32 %0, 43712, %0\n\t"
               "global_load_lds_dwordx4 %0, %7 nt\n\t"
               "s_lshl_b64 vcc, -1, 60\n\t"
               "s_add_u32 m0, %8, 3072\n\t"
               "v_cndmask_b32 %0, %1, %4, vcc\n\t"
               "v_add_u32 %0, 86400, %0\n\t"
               "global_load_lds_dwordx4 %0, %7 nt\n\t"
               "s_add_u32 m0, %8, 4096\n\t"
               "v_add_u32 %0, 129088, %2\n\t"
               "global_load_lds_dwordx4 %0, %7 nt\n\t"
               "s_lshl_b64 vcc, -1, 16\n\t"
               "s_add_u32 m0, %8, 5120\n\t"
               "v_cndmask_b32 %0, %3, %6, vcc\n\t"
               "v_add_u32 %0, 130112, %0\n\t"
               "global_load_lds_dwordx4 %0, %7 nt\n\t"
               "s_lshl_b64 vcc, -1, 36\n\t"
               "s_add_u32 m0, %8, 6144\n\t"
               "v_cndmask_b32 %0, %1, %4, vcc\n\t"
               "v_add_u32 %0, 172800, %0\n\t"
               "global_load_lds_dwordx4 %0, %7 nt\n\t"
               "s_lshl_b64 vcc, -1, 56\n\t"
               "s_add_u32 m0, %8, 7168\n\t"
               "v_cndmask_b32 %0, %2, %5, vcc\n\t"
               "v_add_u32 %0, 215488, %0\n\t"
               "global_load_lds_dwordx4 %0, %7 nt\n\t"
               "s_add_u32 m0, %8, 8192\n\t"
               "v_add_u32 %0, 258176, %3\n\t"
               "global_load_lds_dwordx4 %0, %7 nt\n\t"
               "s_mov_b64 exec, 0xfff\n\t"
               "s_add_u32 m0, %8, 9216\n\t"
               "v_add_u32 %0, 259200, %1\n\t"
               "global_load_lds_dwordx4 %0, %7 nt\n\t"
               "s_mov_b64 exec, -1\n\t"
               : "=&v"(xt) : "v"(xa[0]), "v"(xa[1]), "v"(xa[2]), "v"(xb2[0]), "v"(xb2[1]), "v"(xb2[2]), "s"(x + tw), "s"(xd) : "memory", "scc", "vcc");
  {
    const u64 w03 = wid == 0 ? (u64)wq : (u64)wo, w12 = (u64)wkv + (wid & 2) * 9216, wsel = ((wid ^ (wid >> 1)) & 1) ? w12 : w03;
    const char* wb = (const char*)wsel;
    asm volatile("global_load_dword a244, %0, %1 " :: "v"(min(r * 5 + min(lane, 4), 143) * 128), "s"(wb) : "memory", "a244");
  }
  asm volatile("s_mov_b32 m0, %1" : "+v"(lane) : "s"(m0save));
  const int i16 = lane & 15, g = lane >> 4, q4 = i16 >> 2, p4 = i16 & 3, grp = blk & 7;
  const int role = (wid == 1 && r >= 14 && grp == 7) ? 2 : 0, fr = max(r - 14, 0), fo = (fr * 11) >> 5, fs = fr - 3 * fo;
  u32x16 va, vb, sa; u32x8 sb; u32x4 vc;
  va[0] = lane * 16;
  const int t0 = 8 * g + q4;
  auto pool_addr = [&](int tau) { const int T = 49 * wid + tau; return L_X + T * 192 + (((p4 >> 1) ^ (((T >> 2) + 1) & 3)) << 4) + (p4 & 1) * 8; };
  va[1] = pool_addr(t0); vc[3] = pool_addr(t0 + 4); va[2] = pool_addr(min(t0 + 32, 48)); va[3] = pool_addr(min(t0 + 36, 48));
  va[4] = tile * 6144 + (4 * p + wid) * 192 + ((g ^ ((-p) & 3)) << 4);
  va[5] = WS_FLAGS + (wid * (NREP / 4) + lane) * 2048 + ((tile & 3) * 64 + (tile >> 2) * 8 + p) * 8;
  va[6] = L_X + (48 * wid + i16) * 192 + ((g ^ ((q4 + 1) & 3)) << 4);
  va[7] = i16 < 4 ? L_X + (192 + p4) * 192 + ((g ^ 1) << 4) : L_ZERO + g * 16;
  va[8] = i16 * 192 + ((g ^ ((-q4) & 3)) << 4);
  va[9] = (4 * g + q4) * 192 + (((p4 >> 1) ^ ((-g) & 3)) << 4) + (p4 & 1) * 8;
  va[10] = WS_W + 18432 + fr * 1024 + lane * 16;
  va[11] = WS_WFL + 256 + fr * 8;
  va[12] = L_WF + (8 * g + q4) * 64 + p4 * 16 + (fo & 1) * 8;
  va[13] = p4 <= 1 ? 0x42444244u : 0u;
  va[14] = i16 < 4 ? 0u : 0xf1800000u;
  va[15] = g < 2 ? 0x3f803f80u : g == 2 ? 0x3f80u : 0u;
#pragma unroll
  for (int b = 0; b < 3; ++b) vb[b] = token_off(48 * wid + 16 * b + i16) + g * 16;
  const int xq = min(lane / 12, 3), xc = lane - 12 * (lane / 12);
  vb[3] = token_off(192 + xq) + xc * 16;
  vb[4] = L_X + (xc >> 2) * 2048 + (16 * (xc & 3) + xq) * 16;
  vb[5] = L_X + 6144 + (16 * (xc & 3) + xq) * 16;
  vb[6] = g < 2 ? 0x3f803f80u : 0u;
  vb[7] = (lane & 1) ? 0xEEEEEEEEu : 0x44444444u;
#pragma unroll
  for (int ks = 0; ks < 3; ++ks) {
    const int cq = 12 * (i16 & 1) + 4 * ks + g, ck = 4 * ks + g, b1 = (i16 >> 1) & 1;
    vb[8 + ks] = L_WQ + (4 * half + (i16 >> 3)) * 3072 + (cq >> 3) * 1024 + (4 * (q4 & 1) + b1) * 128 + (((cq & 7) ^ ((-q4) & 3)) << 4);
    vb[11 + ks] = L_WKV + (6 * par + (i16 >> 3)) * 3072 + (ck >> 3) * 1024 + (i16 & 7) * 128 + (((ck & 7) ^ (4 * (q4 & 1) + 2 * ((i16 >> 3) ^ par) + b1)) << 4);
  }
  vb[14] = 79872 + (9 * par + 2 * half) * 1024 + lane * 16;
  const unsigned seg = (lane >> 3) * 384;
  vb[15] = seg + (((lane & 7) ^ (4 * (lane >> 5) + 2 * (par ^ half) + (g & 1))) << 4) + wid * 3072;
  vc[0] = seg + (((lane & 7) ^ ((-(2 * par + (lane >> 5))) & 3)) << 4);
  vc[1] = (32 * fs + 16 * (q4 & 1) + 4 * (i16 >> 3) + p4) * 384 + 192 + g * 16;
  vc[2] = (lane >> 2) * 192 + (fo >> 1) * 64 + (lane & 3) * 16;
  sb[0] = (unsigned)(uintptr_t)wq; sb[1] = (unsigned)((uintptr_t)wq >> 32); sb[2] = (unsigned)(uintptr_t)wkv; sb[3] = (unsigned)((uintptr_t)wkv >> 32);
  sb[4] = (unsigned)(uintptr_t)wo; sb[5] = (unsigned)((uintptr_t)wo >> 32); sb[6] = sb[7] = 0;
  char* outb = out + tok0;
  const char* fl = ws + WS_FLAGS + (blk % NREP) * 2048 + wid * 512;
  const char* wfl = ws + WS_WFL;
  const unsigned wstart = (9 * half + 4 * par) * 1024;
  const char* wd = ws + WS_W + wstart + 2048;
  sa[0] = (unsigned)(uintptr_t)ws; sa[1] = (unsigned)((uintptr_t)ws >> 32); sa[2] = (unsigned)(uintptr_t)outb; sa[3] = (unsigned)((uintptr_t)outb >> 32);
  sa[4] = wid; sa[5] = blk + wid; sa[6] = (unsigned)(uintptr_t)fl; sa[7] = (unsigned)((uintptr_t)fl >> 32);
  sa[8] = (unsigned)(uintptr_t)wfl; sa[9] = (unsigned)((uintptr_t)wfl >> 32); sa[10] = (unsigned)(uintptr_t)wd; sa[11] = (unsigned)((uintptr_t)wd >> 32);
  sa[12] = role; sa[13] = wstart; sa[14] = sa[15] = 0;
  unsigned fail;
  asm volatile(
    "s_mov_b32 s36, m0\n"
    "s_mov_b32 s62, 0x3b44ef58\n"
    "v_mov_b32 v24, 0x3f803f80\n"
    "v_mov_b32 v28, v231\n"
    "v_accvgpr_write_b32 a240, v229\n"
    "v_mov_b32 v164, v230\n"
    "v_mov_b32 v25, 0x3f803f80\n"
    "v_mov_b32 v29, v238\n"
    "v_accvgpr_write_b32 a241, v229\n"
    "v_mov_b32 v165, v230\n"
    "v_mov_b32 v26, 0x3f803f80\n"
    "v_mov_b32 v30, v238\n"
    "v_accvgpr_write_b32 a242, v229\n"
    "v_mov_b32 v166, v230\n"
    "v_mov_b32 v27, 0x3f803f80\n"
    "v_mov_b32 v31, v238\n"
    "v_accvgpr_write_b32 a243, v229\n"
    "v_mov_b32 v167, v230\n"
    "v_lshrrev_b32 v231, 1, v216\n"
    "v_add_u32 v238, 98304, v216\n"
    "v_add_u32 v78, 79872, v216\n"
    "v_mov_b32 v230, 213248\n"
    "v_mov_b64 v[0:1], 0\n"
    "v_lshrrev_b32 v2, 2, v216\n"
    "v_add_u32 v2, 154368, v2\n"
    "ds_write_b32 v2, v0 offset:0\n"
    "v_xor_b32 v56, 32, v243\n"
    "v_xor_b32 v57, 32, v244\n"
    "v_xor_b32 v58, 32, v245\n"
    "s_cmp_ge_u32 s88, 2\n"
    "s_mov_b32 s38, 6144\n"
    "s_cmov_b32 s38, -5888\n"
    "s_mov_b32 s39, 1024\n"
    "s_cmov_b32 s39, -1016\n"
    "v_add_u32 v80, s38, v240\n"
    "v_add_u32 v81, s38, v241\n"
    "v_add_u32 v82, s38, v242\n"
    "v_add_u32 v79, s39, v246\n"
    "v_xor_b32 v59, 32, v217\n"
    "v_xor_b32 v60, 32, v251\n"
    "v_xor_b32 v61, 32, v218\n"
    "v_xor_b32 v71, 32, v219\n"
    "s_mul_i32 s40, s88, 6144\n"
    "s_mov_b32 s37, 0x40000\n"
    "s_mov_b32 s41, 0\n"
    "s_mov_b32 s74, 0x53800000\n"
    "s_mov_b32 s60, 0x1c800000\n"
    "s_mov_b32 s61, 0x67800000\n"
    "s_add_u32 s38, s88, 1\n"
    "s_and_b32 s38, s38, 3\n"
    "s_mul_i32 s38, s38, 6144\n"
    "v_add_u32 v168, s38, v224\n"
    "v_add_u32 v169, s38, v225\n"
    "v_xor_b32 v170, 32, v169\n"
    "s_add_u32 s38, s88, 2\n"
    "s_and_b32 s38, s38, 3\n"
    "s_mul_i32 s38, s38, 6144\n"
    "v_add_u32 v171, s38, v224\n"
    "v_add_u32 v172, s38, v225\n"
    "v_xor_b32 v173, 32, v172\n"
    "s_add_u32 s38, s88, 3\n"
    "s_and_b32 s38, s38, 3\n"
    "s_mul_i32 s38, s38, 6144\n"
    "v_add_u32 v174, s38, v224\n"
    "v_add_u32 v175, s38, v225\n"
    "v_xor_b32 v176, 32, v175\n"
    "s_add_u32 s38, s88, 4\n"
    "s_and_b32 s38, s38, 3\n"
    "s_mul_i32 s38, s38, 6144\n"
    "v_add_u32 v177, s38, v224\n"
    "v_add_u32 v178, s38, v225\n"
    "v_xor_b32 v179, 32, v178\n"
    "v_add_u32 v180, 49152, v168\n"
    "v_add_u32 v181, 49152, v169\n"
    "v_add_u32 v182, 49152, v170\n"
    "v_add_u32 v183, 49152, v171\n"
    "v_add_u32 v184, 49152, v172\n"
    "v_add_u32 v185, 49152, v173\n"
    "v_add_u32 v186, 49152, v174\n"
    "v_add_u32 v187, 49152, v175\n"
    "v_add_u32 v188, 49152, v176\n"
    "v_add_u32 v189, 49152, v177\n"
    "v_add_u32 v190, 49152, v178\n"
    "v_add_u32 v191, 49152, v179\n"
    "s_add_u32 s38, s84, s40\n"
    "s_addc_u32 s39, s85, 0\n"
    "s_add_u32 s42, s38, 3072\n"
    "s_addc_u32 s43, s39, 0\n"
    "s_add_u32 s44, s38, 27648\n"
    "s_addc_u32 s45, s39, 0\n"
    "s_add_u32 s46, s38, 52224\n"
    "s_addc_u32 s47, s39, 0\n"
    "s_add_u32 s48, s38, 76800\n"
    "s_addc_u32 s49, s39, 0\n"
    "s_add_u32 s50, s38, 101376\n"
    "s_addc_u32 s51, s39, 0\n"
    "s_add_u32 s52, s38, 125952\n"
    "s_addc_u32 s53, s39, 0\n"
    "s_add_u32 s54, s38, 150528\n"
    "s_addc_u32 s55, s39, 0\n"
    "s_add_u32 s56, s38, 175104\n"
    "s_addc_u32 s57, s39, 0\n"
    "s_mul_i32 s38, s88, 7168\n"
    "s_add_u32 s38, s38, 116736\n"
    "v_add_u32 v229, s38, v216\n"
    "s_mov_b32 s63, s97\n"
    "s_add_u32 s76, s94, 18432\n"
    "s_addc_u32 s77, s95, 0\n"
    "s_cmp_eq_u32 s96, 2\n"
    "s_cselect_b64 s[78:79], 1, 0\n"
    "s_mov_b32 s41, twins_p0-twins_pc\n"
    "s_cmp_eq_u32 s88, 1\n"
    "s_cmov_b32 s41, twins_p1-twins_pc\n"
    "s_cmp_eq_u32 s88, 2\n"
    "s_cmov_b32 s41, twins_p2-twins_pc\n"
    "s_cmp_eq_u32 s88, 3\n"
    "s_cmov_b32 s41, twins_p3-twins_pc\n"
    "s_getpc_b64 s[38:39]\n"
    "twins_pc:\n"
    "s_add_u32 s38, s38, s41\n"
    "s_addc_u32 s39, s39, 0\n"
    "s_mov_b64 exec, 0\n"
    "s_setpc_b64 s[38:39]\n"
    "twins_ret:\n"
    "s_mov_b64 exec, -1\n"
    "s_branch twins_a\n"
    ".p2align 12\n"
    "twins_a:\n"
    "twins_p0:\n"
    "s_cbranch_execz twins_p4\n"
    "v_mov_b32 v46, 212992\n"
    "global_load_dwordx2 v[44:45], v46, s[84:85] sc1\n"
    "s_and_b32 s38, s88, 1\n"
    "s_mul_i32 s38, s38, 3072\n"
    "s_add_u32 s39, s38, 24576\n"
    "s_cmp_ge_u32 s88, 2\n"
    "s_mov_b32 s41, 0\n"
    "s_cmov_b32 s41, 6272\n"
    "s_mov_b32 s68, 0\n"
    "s_cmov_b32 s68, 7168\n"
    "v_add3_u32 v64, s41, v248, 0\n"
    "s_mov_b32 s41, 128\n"
    "s_cmov_b32 s41, 6400\n"
    "s_mov_b32 s69, 1024\n"
    "s_cmov_b32 s69, 8192\n"
    "v_add3_u32 v65, s41, v248, 0\n"
    "s_mov_b32 s41, 256\n"
    "s_cmov_b32 s41, 12288\n"
    "s_mov_b32 s70, 2048\n"
    "s_cmov_b32 s70, 12288\n"
    "v_add3_u32 v66, s41, v248, 0\n"
    "s_mov_b32 s41, 6144\n"
    "s_cmov_b32 s41, 12416\n"
    "s_mov_b32 s71, 6144\n"
    "s_cmov_b32 s71, 13312\n"
    "v_add3_u32 v67, s41, v248, 0\n"
    "s_mov_b32 s41, 6272\n"
    "s_cmov_b32 s41, 12544\n"
    "s_mov_b32 s72, 7168\n"
    "s_cmov_b32 s72, 14336\n"
    "v_add3_u32 v68, s41, v248, 0\n"
    "s_add_u32 m0, s68, s39\n"
    "v_add_u32 v64, s38, v64\n"
    "global_load_lds_dwordx4 v64, s[28:29] \n"
    "s_add_u32 m0, s69, s39\n"
    "v_add_u32 v65, s38, v65\n"
    "global_load_lds_dwordx4 v65, s[28:29] \n"
    "s_add_u32 m0, s70, s39\n"
    "v_add_u32 v66, s38, v66\n"
    "global_load_lds_dwordx4 v66, s[28:29] \n"
    "s_add_u32 m0, s71, s39\n"
    "v_add_u32 v67, s38, v67\n"
    "global_load_lds_dwordx4 v67, s[28:29] \n"
    "s_add_u32 m0, s72, s39\n"
    "v_add_u32 v68, s38, v68\n"
    "global_load_lds_dwordx4 v68, s[28:29] \n"
    "s_mul_i32 s38, s88, 3072\n"
    "v_and_b32 v69, 64, v247\n"
    "v_cmp_eq_u32 vcc, 0, v69\n"
    "s_add_u32 m0, s38, 43008\n"
    "s_nop 0\n"
    "global_load_lds_dwordx4 v247, s[30:31] offset:0 \n"
    "s_add_u32 m0, s38, 55296\n"
    "v_add_u32 v247, 12288, v247\n"
    "global_load_lds_dwordx4 v247, s[30:31] offset:0 \n"
    "s_add_u32 m0, s38, 67584\n"
    "v_add_u32 v247, 12288, v247\n"
    "global_load_lds_dwordx4 v247, s[30:31] offset:0 \n"
    "s_add_u32 m0, s38, 68480\n"
    "s_nop 0\n"
    "global_load_lds_dwordx4 v247, s[30:31] offset:128 \n"
    "s_add_u32 m0, s38, 56192\n"
    "v_add_u32 v247, -12288, v247\n"
    "global_load_lds_dwordx4 v247, s[30:31] offset:128 \n"
    "s_add_u32 m0, s38, 43904\n"
    "v_add_u32 v247, -12288, v247\n"
    "global_load_lds_dwordx4 v247, s[30:31] offset:128 \n"
    "s_mov_b64 exec, -1\n"
    "s_mov_b32 s41, 0\n"
    "s_waitcnt vmcnt(17) lgkmcnt(0)\n"
    "ds_read_b64_tr_b16 a[96:97], v217 offset:0\n"
    "ds_read_b64_tr_b16 a[98:99], v251 offset:0\n"
    "ds_read_b64_tr_b16 a[104:105], v59 offset:0\n"
    "ds_read_b64_tr_b16 a[106:107], v60 offset:0\n"
    "ds_read_b64_tr_b16 a[112:113], v217 offset:64\n"
    "ds_read_b64_tr_b16 a[114:115], v251 offset:64\n"
    "ds_read_b64_tr_b16 a[120:121], v59 offset:64\n"
    "ds_read_b64_tr_b16 a[122:123], v60 offset:64\n"
    "ds_read_b64_tr_b16 a[128:129], v217 offset:128\n"
    "ds_read_b64_tr_b16 a[130:131], v251 offset:128\n"
    "ds_read_b64_tr_b16 a[136:137], v59 offset:128\n"
    "ds_read_b64_tr_b16 a[138:139], v60 offset:128\n"
    "s_waitcnt lgkmcnt(10)\n"
    "v_mfma_f32_16x16x32_bf16 v[84:87], a[96:99], v[24:27], 0\n"
    "s_waitcnt lgkmcnt(8)\n"
    "v_mfma_f32_16x16x32_bf16 v[88:91], a[104:107], v[24:27], 0\n"
    "s_waitcnt lgkmcnt(6)\n"
    "v_mfma_f32_16x16x32_bf16 v[92:95], a[112:115], v[24:27], 0\n"
    "s_waitcnt lgkmcnt(4)\n"
    "v_mfma_f32_16x16x32_bf16 v[96:99], a[120:123], v[24:27], 0\n"
    "s_waitcnt lgkmcnt(2)\n"
    "v_mfma_f32_16x16x32_bf16 v[100:103], a[128:131], v[24:27], 0\n"
    "s_waitcnt lgkmcnt(0)\n"
    "v_mfma_f32_16x16x32_bf16 v[104:107], a[136:139], v[24:27], 0\n"
    "s_waitcnt vmcnt(13)\n"
    "ds_read_b64_tr_b16 a[100:101], v218 offset:0\n"
    "ds_read_b64_tr_b16 a[102:103], v219 offset:0\n"
    "ds_read_b64_tr_b16 a[108:109], v61 offset:0\n"
    "ds_read_b64_tr_b16 a[110:111], v71 offset:0\n"
    "ds_read_b64_tr_b16 a[116:117], v218 offset:64\n"
    "ds_read_b64_tr_b16 a[118:119], v219 offset:64\n"
    "ds_read_b64_tr_b16 a[124:125], v61 offset:64\n"
    "ds_read_b64_tr_b16 a[126:127], v71 offset:64\n"
    "ds_read_b64_tr_b16 a[132:133], v218 offset:128\n"
    "ds_read_b64_tr_b16 a[134:135], v219 offset:128\n"
    "ds_read_b64_tr_b16 a[140:141], v61 offset:128\n"
    "ds_read_b64_tr_b16 a[142:143], v71 offset:128\n"
    "s_waitcnt lgkmcnt(10)\n"
    "v_mfma_f32_16x16x32_bf16 v[84:87], a[100:103], v[28:31], v[84:87]\n"
    "s_waitcnt lgkmcnt(8)\n"
    "v_mfma_f32_16x16x32_bf16 v[88:91], a[108:111], v[28:31], v[88:91]\n"
    "s_waitcnt lgkmcnt(6)\n"
    "v_mfma_f32_16x16x32_bf16 v[92:95], a[116:119], v[28:31], v[92:95]\n"
    "s_waitcnt lgkmcnt(4)\n"
    "v_mfma_f32_16x16x32_bf16 v[96:99], a[124:127], v[28:31], v[96:99]\n"
    "s_waitcnt lgkmcnt(2)\n"
    "v_mfma_f32_16x16x32_bf16 v[100:103], a[132:135], v[28:31], v[100:103]\n"
    "s_waitcnt lgkmcnt(0)\n"
    "v_mfma_f32_16x16x32_bf16 v[104:107], a[140:143], v[28:31], v[104:107]\n"
    "v_cvt_pk_bf16_f32 v32, v84, v85\n"
    "v_cvt_pk_bf16_f32 v33, v86, v87\n"
    "s_mov_b32 exec_lo, 0x00010001\n"
    "s_mov_b32 exec_hi, 0x00010001\n"
    "v_cvt_pk_bf16_f32 v34, v88, v89\n"
    "v_cvt_pk_bf16_f32 v35, v90, v91\n"
    "global_store_dwordx4 v220, v[32:35], s[84:85] offset:0 sc1\n"
    "v_cvt_pk_bf16_f32 v36, v92, v93\n"
    "v_cvt_pk_bf16_f32 v37, v94, v95\n"
    "v_cvt_pk_bf16_f32 v38, v96, v97\n"
    "v_cvt_pk_bf16_f32 v39, v98, v99\n"
    "global_store_dwordx4 v220, v[36:39], s[84:85] offset:64 sc1\n"
    "v_cvt_pk_bf16_f32 v40, v100, v101\n"
    "v_cvt_pk_bf16_f32 v41, v102, v103\n"
    "v_cvt_pk_bf16_f32 v42, v104, v105\n"
    "v_cvt_pk_bf16_f32 v43, v106, v107\n"
    "global_store_dwordx4 v220, v[40:43], s[84:85] offset:128 sc1\n"
    "s_mov_b64 exec, -1\n"
    "s_waitcnt vmcnt(14)\n"
    "v_readfirstlane_b32 s58, v44\n"
    "v_readfirstlane_b32 s59, v45\n"
    "v_max3_f32 v70, |v84|, |v85|, 0\n"
    "v_max3_f32 v70, |v86|, |v87|, v70\n"
    "v_max3_f32 v70, |v88|, |v89|, v70\n"
    "v_max3_f32 v70, |v90|, |v91|, v70\n"
    "v_max3_f32 v70, |v92|, |v93|, v70\n"
    "v_max3_f32 v70, |v94|, |v95|, v70\n"
    "s_mul_i32 s58, s58, 0x9e3779b1\n"
    "s_mul_i32 s59, s59, 0x85ebca77\n"
    "s_add_u32 s58, s58, 0x632be59b\n"
    "s_add_u32 s59, s59, 0x1b873593\n"
    "v_mov_b32 v62, s58\n"
    "v_mov_b32 v63, s59\n"
    "v_max3_f32 v70, |v96|, |v97|, v70\n"
    "v_max3_f32 v70, |v98|, |v99|, v70\n"
    "v_max3_f32 v70, |v100|, |v101|, v70\n"
    "v_max3_f32 v70, |v102|, |v103|, v70\n"
    "v_max3_f32 v70, |v104|, |v105|, v70\n"
    "v_max3_f32 v70, |v106|, |v107|, v70\n"
    "v_cmp_lt_f32 s[64:65], s74, v70\n"
    "s_cmp_lg_u64 s[64:65], 0\n"
    "s_cbranch_scc1 DG0%=\n"
    "DB0%=:\n"
    "s_cmp_lg_u32 s96, 2\n"
    "s_cbranch_scc1 NV%=\n"
    "global_load_dwordx4 a[192:195], v249, s[30:31] offset:0\n"
    "global_load_dwordx4 a[196:199], v249, s[30:31] offset:64\n"
    "global_load_dwordx4 a[200:203], v249, s[30:31] offset:128\n"
    "global_load_dwordx4 a[204:207], v249, s[30:31] offset:3072\n"
    "global_load_dwordx4 a[208:211], v249, s[30:31] offset:3136\n"
    "global_load_dwordx4 a[212:215], v249, s[30:31] offset:3200\n"
    "s_mov_b32 m0, 98304\n"
    "s_nop 0\n"
    "global_load_lds_dwordx4 v250, s[32:33] offset:0\n"
    "s_mov_b32 m0, 96256\n"
    "s_nop 0\n"
    "global_load_lds_dwordx4 v250, s[32:33] offset:3072\n"
    "s_mov_b32 m0, 100352\n"
    "v_add_u32 v250, 6144, v250\n"
    "global_load_lds_dwordx4 v250, s[32:33] offset:0\n"
    "s_mov_b32 m0, 98304\n"
    "s_nop 0\n"
    "global_load_lds_dwordx4 v250, s[32:33] offset:3072\n"
    "s_mov_b32 m0, 102400\n"
    "v_add_u32 v250, 6144, v250\n"
    "global_load_lds_dwordx4 v250, s[32:33] offset:0\n"
    "s_mov_b32 m0, 100352\n"
    "s_nop 0\n"
    "global_load_lds_dwordx4 v250, s[32:33] offset:3072\n"
    "s_waitcnt vmcnt(15)\n"
    "s_branch NU%=\n"
    "NV%=:\n"
    "s_waitcnt vmcnt(3)\n"
    "NU%=:\n"
    "s_barrier\n"
    "ds_read_b128 a[96:99], v243 offset:0\n"
    "ds_read_b128 a[48:51], v240 offset:0\n"
    "ds_read_b128 a[100:103], v244 offset:0\n"
    "ds_read_b128 a[52:55], v241 offset:0\n"
    "ds_read_b128 a[104:107], v245 offset:0\n"
    "ds_read_b128 a[56:59], v242 offset:0\n"
    "ds_read_b128 a[60:63], v240 offset:256\n"
    "ds_read_b128 a[64:67], v241 offset:256\n"
    "ds_read_b128 a[68:71], v242 offset:256\n"
    "s_waitcnt lgkmcnt(3)\n"
    "v_mfma_f32_16x16x32_bf16 v[0:3], a[48:51], a[96:99], 0\n"
    "ds_read_b128 a[72:75], v80 offset:0\n"
    "ds_read_b128 a[76:79], v81 offset:0\n"
    "v_mfma_f32_16x16x32_bf16 v[0:3], a[52:55], a[100:103], v[0:3]\n"
    "s_waitcnt lgkmcnt(2)\n"
    "v_mfma_f32_16x16x32_bf16 v[0:3], a[56:59], a[104:107], v[0:3]\n"
    "ds_read_b128 a[80:83], v82 offset:0\n"
    "v_mfma_f32_16x16x32_bf16 v[4:7], a[60:63], a[96:99], 0\n"
    "ds_read_b128 a[108:111], v56 offset:6144\n"
    "v_mfma_f32_16x16x32_bf16 v[4:7], a[64:67], a[100:103], v[4:7]\n"
    "ds_read_b128 a[112:115], v57 offset:6144\n"
    "v_mfma_f32_16x16x32_bf16 v[4:7], a[68:71], a[104:107], v[4:7]\n"
    "s_waitcnt lgkmcnt(4)\n"
    "ds_read_b128 a[116:119], v58 offset:6144\n"
    "v_mfma_f32_16x16x32_bf16 v[8:11], a[72:75], a[96:99], 0\n"
    "s_waitcnt lgkmcnt(4)\n"
    "ds_read_b128 a[120:123], v243 offset:12288\n"
    "v_mul_f32 v0, s62, v0\n"
    "v_mfma_f32_16x16x32_bf16 v[8:11], a[76:79], a[100:103], v[8:11]\n"
    "s_waitcnt lgkmcnt(4)\n"
    "ds_read_b128 a[124:127], v244 offset:12288\n"
    "v_mul_f32 v1, s62, v1\n"
    "v_mfma_f32_16x16x32_bf16 v[8:11], a[80:83], a[104:107], v[8:11]\n"
    "s_waitcnt lgkmcnt(4)\n"
    "ds_read_b128 a[128:131], v245 offset:12288\n"
    "v_cvt_pk_bf16_f32 v32, v0, v1\n"
    "v_mfma_f32_16x16x32_bf16 v[12:15], a[48:51], a[108:111], 0\n"
    "s_waitcnt lgkmcnt(4)\n"
    "v_mul_f32 v2, s62, v2\n"
    "v_mul_f32 v3, s62, v3\n"
    "v_mfma_f32_16x16x32_bf16 v[12:15], a[52:55], a[112:115], v[12:15]\n"
    "s_waitcnt lgkmcnt(3)\n"
    "v_cvt_pk_bf16_f32 v33, v2, v3\n"
    "v_mul_f32 v4, s62, v4\n"
    "v_mfma_f32_16x16x32_bf16 v[12:15], a[56:59], a[116:119], v[12:15]\n"
    "v_mul_f32 v5, s62, v5\n"
    "v_cvt_pk_bf16_f32 v34, v4, v5\n"
    "v_mfma_f32_16x16x32_bf16 v[16:19], a[60:63], a[108:111], 0\n"
    "v_mul_f32 v6, s62, v6\n"
    "v_mul_f32 v7, s62, v7\n"
    "v_mfma_f32_16x16x32_bf16 v[16:19], a[64:67], a[112:115], v[16:19]\n"
    "s_cmp_eq_u32 s96, 2\n"
    "v_cvt_pk_bf16_f32 v35, v6, v7\n"
    "v_mul_f32 v8, s62, v8\n"
    "v_mfma_f32_16x16x32_bf16 v[16:19], a[68:71], a[116:119], v[16:19]\n"
    "s_mov_b64 exec, 0x3\n"
    "s_cbranch_scc1 FR%=\n"
    "s_waitcnt vmcnt(0)\n"
    "FR%=:\n"
    "s_waitcnt vmcnt(12)\n"
    "s_barrier\n"
    "global_store_dwordx2 v221, v[62:63], s[84:85] sc1\n"
    "s_mov_b64 exec, -1\n"
    "v_mfma_f32_16x16x32_bf16 v[20:23], a[72:75], a[108:111], 0\n"
    "v_mul_f32 v9, s62, v9\n"
    "v_cvt_pk_bf16_f32 v36, v8, v9\n"
    "v_mfma_f32_16x16x32_bf16 v[20:23], a[76:79], a[112:115], v[20:23]\n"
    "v_mul_f32 v10, s62, v10\n"
    "v_mul_f32 v11, s62, v11\n"
    "v_mfma_f32_16x16x32_bf16 v[20:23], a[80:83], a[116:119], v[20:23]\n"
    "s_waitcnt lgkmcnt(0)\n"
    "v_cvt_pk_bf16_f32 v37, v10, v11\n"
    "v_mul_f32 v12, s62, v12\n"
    "v_mfma_f32_16x16x32_bf16 v[0:3], a[48:51], a[120:123], 0\n"
    "v_mul_f32 v13, s62, v13\n"
    "v_cvt_pk_bf16_f32 v40, v12, v13\n"
    "v_mfma_f32_16x16x32_bf16 v[0:3], a[52:55], a[124:127], v[0:3]\n"
    "v_mul_f32 v14, s62, v14\n"
    "v_mul_f32 v15, s62, v15\n"
    "v_mfma_f32_16x16x32_bf16 v[0:3], a[56:59], a[128:131], v[0:3]\n"
    "v_cvt_pk_bf16_f32 v41, v14, v15\n"
    "v_mul_f32 v16, s62, v16\n"
    "v_mfma_f32_16x16x32_bf16 v[4:7], a[60:63], a[120:123], 0\n"
    "v_mul_f32 v17, s62, v17\n"
    "v_cvt_pk_bf16_f32 v42, v16, v17\n"
    "v_mfma_f32_16x16x32_bf16 v[4:7], a[64:67], a[124:127], v[4:7]\n"
    "v_mul_f32 v18, s62, v18\n"
    "v_mul_f32 v19, s62, v19\n"
    "v_mfma_f32_16x16x32_bf16 v[4:7], a[68:71], a[128:131], v[4:7]\n"
    "v_cvt_pk_bf16_f32 v43, v18, v19\n"
    "v_mul_f32 v20, s62, v20\n"
    "v_mfma_f32_16x16x32_bf16 v[8:11], a[72:75], a[120:123], 0\n"
    "v_mul_f32 v21, s62, v21\n"
    "v_cvt_pk_bf16_f32 v44, v20, v21\n"
    "v_mfma_f32_16x16x32_bf16 v[8:11], a[76:79], a[124:127], v[8:11]\n"
    "v_mul_f32 v22, s62, v22\n"
    "v_mul_f32 v23, s62, v23\n"
    "v_mfma_f32_16x16x32_bf16 v[8:11], a[80:83], a[128:131], v[8:11]\n"
    "v_cvt_pk_bf16_f32 v45, v22, v23\n"
    "v_mul_f32 v0, s62, v0\n"
    "s_nop 3\n"
    "v_mul_f32 v1, s62, v1\n"
    "v_cvt_pk_bf16_f32 v48, v0, v1\n"
    "v_mul_f32 v2, s62, v2\n"
    "v_mul_f32 v3, s62, v3\n"
    "v_cvt_pk_bf16_f32 v49, v2, v3\n"
    "v_mul_f32 v4, s62, v4\n"
    "v_mul_f32 v5, s62, v5\n"
    "v_cvt_pk_bf16_f32 v50, v4, v5\n"
    "v_mul_f32 v6, s62, v6\n"
    "v_mul_f32 v7, s62, v7\n"
    "v_cvt_pk_bf16_f32 v51, v6, v7\n"
    "v_mul_f32 v8, s62, v8\n"
    "v_mul_f32 v9, s62, v9\n"
    "v_cvt_pk_bf16_f32 v52, v8, v9\n"
    "v_mul_f32 v10, s62, v10\n"
    "v_mul_f32 v11, s62, v11\n"
    "v_cvt_pk_bf16_f32 v53, v10, v11\n"
    "ds_write_b128 v246, v[32:35] offset:0\n"
    "ds_write_b64 v79, v[36:37] offset:0\n"
    "ds_write_b128 v246, v[40:43] offset:3072\n"
    "ds_write_b64 v79, v[44:45] offset:3072\n"
    "ds_write_b128 v246, v[48:51] offset:6144\n"
    "ds_write_b64 v79, v[52:53] offset:6144\n"
    "v_mov_b32 v240, v62\n"
    "v_mov_b32 v241, v63\n"
    "s_waitcnt lgkmcnt(0)\n"
    "s_barrier\n"
    "CM%=:\n"
    "ds_read_b128 a[96:99], v78 offset:0\n"
    "ds_read_b128 a[144:147], v222 offset:0\n"
    "ds_read_b128 a[100:103], v78 offset:1024\n"
    "ds_read_b128 a[148:151], v222 offset:64\n"
    "ds_read_b128 a[104:107], v78 offset:2048\n"
    "ds_read_b128 a[152:155], v222 offset:128\n"
    "ds_read_b128 a[108:111], v78 offset:3072\n"
    "ds_read_b128 a[112:115], v78 offset:4096\n"
    "ds_read_b128 a[116:119], v78 offset:5120\n"
    "s_waitcnt lgkmcnt(3)\n"
    "v_mfma_f32_16x16x32_bf16 v[0:3], a[96:99], a[144:147], 0\n"
    "ds_read_b128 a[120:123], v78 offset:6144\n"
    "ds_read_b128 a[124:127], v78 offset:7168\n"
    "v_mfma_f32_16x16x32_bf16 v[0:3], a[100:103], a[148:151], v[0:3]\n"
    "s_waitcnt lgkmcnt(2)\n"
    "v_mfma_f32_16x16x32_bf16 v[0:3], a[104:107], a[152:155], v[0:3]\n"
    "global_load_dwordx2 v[72:73], v231, s[90:91] sc1\n"
    "ds_read_b128 a[128:131], v78 offset:8192\n"
    "v_mfma_f32_16x16x32_bf16 v[4:7], a[108:111], a[144:147], 0\n"
    "ds_read_b128 a[132:135], v78 offset:9216\n"
    "v_mfma_f32_16x16x32_bf16 v[4:7], a[112:115], a[148:151], v[4:7]\n"
    "ds_read_b128 a[136:139], v78 offset:10240\n"
    "v_mfma_f32_16x16x32_bf16 v[4:7], a[116:119], a[152:155], v[4:7]\n"
    "s_waitcnt lgkmcnt(4)\n"
    "ds_read_b128 a[140:143], v78 offset:11264\n"
    "v_mfma_f32_16x16x32_bf16 v[8:11], a[120:123], a[144:147], 0\n"
    "s_waitcnt lgkmcnt(4)\n"
    "ds_read_b128 a[216:219], v78 offset:12288\n"
    "v_cvt_pk_bf16_f32 v32, v0, v1\n"
    "v_mfma_f32_16x16x32_bf16 v[8:11], a[124:127], a[148:151], v[8:11]\n"
    "s_waitcnt lgkmcnt(4)\n"
    "ds_read_b128 a[220:223], v78 offset:13312\n"
    "v_cvt_pk_bf16_f32 v33, v2, v3\n"
    "v_mfma_f32_16x16x32_bf16 v[8:11], a[128:131], a[152:155], v[8:11]\n"
    "s_waitcnt lgkmcnt(4)\n"
    "ds_read_b128 a[224:227], v78 offset:14336\n"
    "v_cvt_pk_bf16_f32 v34, v4, v5\n"
    "v_mfma_f32_16x16x32_bf16 v[12:15], a[132:135], a[144:147], 0\n"
    "s_waitcnt lgkmcnt(4)\n"
    "ds_read_b128 a[228:231], v78 offset:15360\n"
    "v_cvt_pk_bf16_f32 v35, v6, v7\n"
    "v_mfma_f32_16x16x32_bf16 v[12:15], a[136:139], a[148:151], v[12:15]\n"
    "s_waitcnt lgkmcnt(4)\n"
    "ds_read_b128 a[232:235], v78 offset:16384\n"
    "v_accvgpr_write_b32 a0, v32\n"
    "v_mfma_f32_16x16x32_bf16 v[12:15], a[140:143], a[152:155], v[12:15]\n"
    "s_waitcnt lgkmcnt(4)\n"
    "ds_read_b128 a[236:239], v78 offset:17408\n"
    "v_cvt_pk_bf16_f32 v36, v8, v9\n"
    "v_mfma_f32_16x16x32_bf16 v[16:19], a[216:219], a[144:147], 0\n"
    "s_waitcnt lgkmcnt(4)\n"
    "ds_read_b128 a[156:159], v222 offset:3072\n"
    "v_cvt_pk_bf16_f32 v37, v10, v11\n"
    "v_mfma_f32_16x16x32_bf16 v[16:19], a[220:223], a[148:151], v[16:19]\n"
    "s_waitcnt lgkmcnt(4)\n"
    "ds_read_b128 a[160:163], v222 offset:3136\n"
    "v_accvgpr_write_b32 a1, v33\n"
    "v_mfma_f32_16x16x32_bf16 v[16:19], a[224:227], a[152:155], v[16:19]\n"
    "s_waitcnt lgkmcnt(4)\n"
    "global_load_dwordx2 v[74:75], v231, s[90:91] sc1\n"
    "ds_read_b128 a[164:167], v222 offset:3200\n"
    "v_mfma_f32_16x16x32_bf16 v[20:23], a[228:231], a[144:147], 0\n"
    "s_waitcnt lgkmcnt(4)\n"
    "ds_read_b128 a[168:171], v222 offset:6144\n"
    "v_cvt_pk_bf16_f32 v38, v12, v13\n"
    "v_mfma_f32_16x16x32_bf16 v[20:23], a[232:235], a[148:151], v[20:23]\n"
    "s_waitcnt lgkmcnt(4)\n"
    "ds_read_b128 a[172:175], v222 offset:6208\n"
    "v_cvt_pk_bf16_f32 v39, v14, v15\n"
    "v_mfma_f32_16x16x32_bf16 v[20:23], a[236:239], a[152:155], v[20:23]\n"
    "s_waitcnt lgkmcnt(4)\n"
    "ds_read_b128 a[176:179], v222 offset:6272\n"
    "v_accvgpr_write_b32 a2, v34\n"
    "v_mfma_f32_16x16x32_bf16 v[0:3], a[96:99], a[156:159], 0\n"
    "s_waitcnt lgkmcnt(4)\n"
    "ds_read_b128 a[180:183], v223 offset:0\n"
    "v_accvgpr_write_b32 a3, v35\n"
    "v_mfma_f32_16x16x32_bf16 v[0:3], a[100:103], a[160:163], v[0:3]\n"
    "s_waitcnt lgkmcnt(4)\n"
    "ds_read_b128 a[184:187], v223 offset:64\n"
    "v_cvt_pk_bf16_f32 v32, v16, v17\n"
    "v_mfma_f32_16x16x32_bf16 v[0:3], a[104:107], a[164:167], v[0:3]\n"
    "ds_read_b128 a[188:191], v223 offset:128\n"
    "v_cvt_pk_bf16_f32 v33, v18, v19\n"
    "v_mfma_f32_16x16x32_bf16 v[4:7], a[108:111], a[156:159], 0\n"
    "v_cvt_pk_bf16_f32 v34, v20, v21\n"
    "v_cvt_pk_bf16_f32 v35, v22, v23\n"
    "v_mfma_f32_16x16x32_bf16 v[4:7], a[112:115], a[160:163], v[4:7]\n"
    "v_accvgpr_write_b32 a4, v36\n"
    "v_accvgpr_write_b32 a5, v37\n"
    "v_mfma_f32_16x16x32_bf16 v[4:7], a[116:119], a[164:167], v[4:7]\n"
    "v_cvt_pk_bf16_f32 v36, v0, v1\n"
    "v_cvt_pk_bf16_f32 v37, v2, v3\n"
    "v_mfma_f32_16x16x32_bf16 v[8:11], a[120:123], a[156:159], 0\n"
    "v_accvgpr_write_b32 a6, v38\n"
    "v_accvgpr_write_b32 a7, v39\n"
    "v_mfma_f32_16x16x32_bf16 v[8:11], a[124:127], a[160:163], v[8:11]\n"
    "v_accvgpr_write_b32 a8, v32\n"
    "v_accvgpr_write_b32 a9, v33\n"
    "v_mfma_f32_16x16x32_bf16 v[8:11], a[128:131], a[164:167], v[8:11]\n"
    "global_load_dwordx2 v[76:77], v231, s[90:91] sc1\n"
    "v_cvt_pk_bf16_f32 v38, v4, v5\n"
    "v_cvt_pk_bf16_f32 v39, v6, v7\n"
    "v_mfma_f32_16x16x32_bf16 v[12:15], a[132:135], a[156:159], 0\n"
    "v_accvgpr_write_b32 a10, v34\n"
    "v_accvgpr_write_b32 a11, v35\n"
    "v_mfma_f32_16x16x32_bf16 v[12:15], a[136:139], a[160:163], v[12:15]\n"
    "v_accvgpr_write_b32 a12, v36\n"
    "v_accvgpr_write_b32 a13, v37\n"
    "v_mfma_f32_16x16x32_bf16 v[12:15], a[140:143], a[164:167], v[12:15]\n"
    "v_cvt_pk_bf16_f32 v32, v8, v9\n"
    "v_cvt_pk_bf16_f32 v33, v10, v11\n"
    "v_mfma_f32_16x16x32_bf16 v[16:19], a[216:219], a[156:159], 0\n"
    "v_accvgpr_write_b32 a14, v38\n"
    "v_accvgpr_write_b32 a15, v39\n"
    "v_mfma_f32_16x16x32_bf16 v[16:19], a[220:223], a[160:163], v[16:19]\n"
    "v_accvgpr_write_b32 a16, v32\n"
    "v_accvgpr_write_b32 a17, v33\n"
    "v_mfma_f32_16x16x32_bf16 v[16:19], a[224:227], a[164:167], v[16:19]\n"
    "s_waitcnt vmcnt(2)\n"
    "v_cmp_ne_u64 vcc, s[58:59], v[72:73]\n"
    "v_mfma_f32_16x16x32_bf16 v[20:23], a[228:231], a[156:159], 0\n"
    "s_and_b32 s38, vcc_lo, 0xff\n"
    "v_cvt_pk_bf16_f32 v34, v12, v13\n"
    "v_cvt_pk_bf16_f32 v35, v14, v15\n"
    "v_mfma_f32_16x16x32_bf16 v[20:23], a[232:235], a[160:163], v[20:23]\n"
    "s_cbranch_scc1 Eu%=\n"
    "s_add_u32 m0, s40, 3072\n"
    "s_nop 0\n"
    "global_load_lds_dwordx4 v216, s[42:43] offset:-3072 sc1\n"
    "global_load_lds_dwordx4 v216, s[42:43] offset:-2048 sc1\n"
    "global_load_lds_dwordx4 v216, s[42:43] offset:-1024 sc1\n"
    "global_load_lds_dwordx4 v216, s[42:43] offset:0 sc1\n"
    "global_load_lds_dwordx4 v216, s[42:43] offset:1024 sc1\n"
    "global_load_lds_dwordx4 v216, s[42:43] offset:2048 sc1\n"
    "s_mov_b32 s41, 1\n"
    "s_branch Ev%=\n"
    "Eu%=:\n"
    "global_load_dwordx2 v[72:73], v231, s[90:91] sc1\n"
    "Ev%=:\n"
    "v_mfma_f32_16x16x32_bf16 v[20:23], a[236:239], a[164:167], v[20:23]\n"
    "s_waitcnt lgkmcnt(0)\n"
    "v_cvt_pk_bf16_f32 v36, v16, v17\n"
    "v_cvt_pk_bf16_f32 v37, v18, v19\n"
    "v_mfma_f32_16x16x32_bf16 v[0:3], a[96:99], a[168:171], 0\n"
    "v_accvgpr_write_b32 a18, v34\n"
    "v_accvgpr_write_b32 a19, v35\n"
    "v_mfma_f32_16x16x32_bf16 v[0:3], a[100:103], a[172:175], v[0:3]\n"
    "v_accvgpr_write_b32 a20, v36\n"
    "v_accvgpr_write_b32 a21, v37\n"
    "v_mfma_f32_16x16x32_bf16 v[0:3], a[104:107], a[176:179], v[0:3]\n"
    "v_cvt_pk_bf16_f32 v38, v20, v21\n"
    "v_cvt_pk_bf16_f32 v39, v22, v23\n"
    "v_mfma_f32_16x16x32_bf16 v[4:7], a[108:111], a[168:171], 0\n"
    "v_accvgpr_write_b32 a22, v38\n"
    "v_accvgpr_write_b32 a23, v39\n"
    "v_mfma_f32_16x16x32_bf16 v[4:7], a[112:115], a[172:175], v[4:7]\n"
    "v_mfma_f32_16x16x32_bf16 v[4:7], a[116:119], a[176:179], v[4:7]\n"
    "v_mfma_f32_16x16x32_bf16 v[8:11], a[120:123], a[168:171], 0\n"
    "v_cvt_pk_bf16_f32 v32, v0, v1\n"
    "v_cvt_pk_bf16_f32 v33, v2, v3\n"
    "v_mfma_f32_16x16x32_bf16 v[8:11], a[124:127], a[172:175], v[8:11]\n"
    "v_accvgpr_write_b32 a24, v32\n"
    "v_accvgpr_write_b32 a25, v33\n"
    "v_mfma_f32_16x16x32_bf16 v[8:11], a[128:131], a[176:179], v[8:11]\n"
    "v_mfma_f32_16x16x32_bf16 v[12:15], a[132:135], a[168:171], 0\n"
    "v_cvt_pk_bf16_f32 v34, v4, v5\n"
    "v_cvt_pk_bf16_f32 v35, v6, v7\n"
    "v_mfma_f32_16x16x32_bf16 v[12:15], a[136:139], a[172:175], v[12:15]\n"
    "s_cmp_lg_u32 s41, 0\n"
    "v_accvgpr_write_b32 a26, v34\n"
    "v_accvgpr_write_b32 a27, v35\n"
    "v_mfma_f32_16x16x32_bf16 v[12:15], a[140:143], a[176:179], v[12:15]\n"
    "s_cbranch_scc1 Ex%=\n"
    "s_waitcnt vmcnt(2)\n"
    "v_cmp_ne_u64 vcc, s[58:59], v[74:75]\n"
    "s_and_b32 s38, vcc_lo, 0xff\n"
    "s_cbranch_scc1 Ew%=\n"
    "s_add_u32 m0, s40, 3072\n"
    "s_nop 0\n"
    "global_load_lds_dwordx4 v216, s[42:43] offset:-3072 sc1\n"
    "global_load_lds_dwordx4 v216, s[42:43] offset:-2048 sc1\n"
    "global_load_lds_dwordx4 v216, s[42:43] offset:-1024 sc1\n"
    "global_load_lds_dwordx4 v216, s[42:43] offset:0 sc1\n"
    "global_load_lds_dwordx4 v216, s[42:43] offset:1024 sc1\n"
    "global_load_lds_dwordx4 v216, s[42:43] offset:2048 sc1\n"
    "s_mov_b32 s41, 1\n"
    "s_branch Ex%=\n"
    "Ew%=:\n"
    "global_load_dwordx2 v[74:75], v231, s[90:91] sc1\n"
    "Ex%=:\n"
    "v_mfma_f32_16x16x32_bf16 v[16:19], a[216:219], a[168:171], 0\n"
    "v_cvt_pk_bf16_f32 v36, v8, v9\n"
    "v_cvt_pk_bf16_f32 v37, v10, v11\n"
    "v_mfma_f32_16x16x32_bf16 v[16:19], a[220:223], a[172:175], v[16:19]\n"
    "v_accvgpr_write_b32 a28, v36\n"
    "v_accvgpr_write_b32 a29, v37\n"
    "v_mfma_f32_16x16x32_bf16 v[16:19], a[224:227], a[176:179], v[16:19]\n"
    "v_cvt_pk_bf16_f32 v38, v12, v13\n"
    "v_cvt_pk_bf16_f32 v39, v14, v15\n"
    "v_mfma_f32_16x16x32_bf16 v[20:23], a[228:231], a[168:171], 0\n"
    "v_accvgpr_write_b32 a30, v38\n"
    "v_accvgpr_write_b32 a31, v39\n"
    "v_mfma_f32_16x16x32_bf16 v[20:23], a[232:235], a[172:175], v[20:23]\n"
    "v_mfma_f32_16x16x32_bf16 v[20:23], a[236:239], a[176:179], v[20:23]\n"
    "s_nop 5\n"
    "v_cvt_pk_bf16_f32 v32, v16, v17\n"
    "v_cvt_pk_bf16_f32 v33, v18, v19\n"
    "v_cvt_pk_bf16_f32 v34, v20, v21\n"
    "v_cvt_pk_bf16_f32 v35, v22, v23\n"
    "v_accvgpr_write_b32 a32, v32\n"
    "v_accvgpr_write_b32 a33, v33\n"
    "v_accvgpr_write_b32 a34, v34\n"
    "v_accvgpr_write_b32 a35, v35\n"
    "s_cmp_lg_u32 s41, 0\n"
    "s_cbranch_scc1 G0J%=\n"
    "PLA%=:\n"
    "s_waitcnt vmcnt(2)\n"
    "v_cmp_ne_u64 vcc, s[58:59], v[76:77]\n"
    "s_and_b32 s38, vcc_lo, 0xff\n"
    "s_cbranch_scc0 G0I%=\n"
    "global_load_dwordx2 v[76:77], v231, s[90:91] sc1\n"
    "s_waitcnt vmcnt(2)\n"
    "v_cmp_ne_u64 vcc, s[58:59], v[72:73]\n"
    "s_and_b32 s38, vcc_lo, 0xff\n"
    "s_cbranch_scc0 G0I%=\n"
    "global_load_dwordx2 v[72:73], v231, s[90:91] sc1\n"
    "s_waitcnt vmcnt(2)\n"
    "v_cmp_ne_u64 vcc, s[58:59], v[74:75]\n"
    "s_and_b32 s38, vcc_lo, 0xff\n"
    "s_cbranch_scc0 G0I%=\n"
    "s_cmp_eq_u32 s37, 0\n"
    "s_cbranch_scc1 G0I%=\n"
    "s_sub_u32 s37, s37, 1\n"
    "global_load_dwordx2 v[74:75], v231, s[90:91] sc1\n"
    "s_branch PLA%=\n"
    "G0I%=:\n"
    "s_add_u32 m0, s40, 3072\n"
    "s_nop 0\n"
    "global_load_lds_dwordx4 v216, s[42:43] offset:-3072 sc1\n"
    "global_load_lds_dwordx4 v216, s[42:43] offset:-2048 sc1\n"
    "global_load_lds_dwordx4 v216, s[42:43] offset:-1024 sc1\n"
    "global_load_lds_dwordx4 v216, s[42:43] offset:0 sc1\n"
    "global_load_lds_dwordx4 v216, s[42:43] offset:1024 sc1\n"
    "global_load_lds_dwordx4 v216, s[42:43] offset:2048 sc1\n"
    "G0J%=:\n"
    "s_and_b32 s38, vcc_lo, 0xff00\n"
    "s_cbranch_scc0 G1J%=\n"
    "global_load_dwordx2 v[76:77], v231, s[90:91] sc1\n"
    "Lb%=:\n"
    "s_waitcnt vmcnt(0)\n"
    "v_cmp_ne_u64 vcc, s[58:59], v[76:77]\n"
    "s_and_b32 s38, vcc_lo, 0xff00\n"
    "s_cbranch_scc0 Kb%=\n"
    "s_cmp_eq_u32 s37, 0\n"
    "s_cbranch_scc1 Kb%=\n"
    "s_sub_u32 s37, s37, 1\n"
    "s_sleep 2\n"
    "global_load_dwordx2 v[76:77], v231, s[90:91] sc1\n"
    "s_waitcnt vmcnt(0)\n"
    "s_branch Lb%=\n"
    "Kb%=:\n"
    "G1J%=:\n"
    "s_add_u32 m0, s40, 27648\n"
    "s_nop 0\n"
    "global_load_lds_dwordx4 v216, s[44:45] offset:-3072 sc1\n"
    "global_load_lds_dwordx4 v216, s[44:45] offset:-2048 sc1\n"
    "global_load_lds_dwordx4 v216, s[44:45] offset:-1024 sc1\n"
    "global_load_lds_dwordx4 v216, s[44:45] offset:0 sc1\n"
    "global_load_lds_dwordx4 v216, s[44:45] offset:1024 sc1\n"
    "global_load_lds_dwordx4 v216, s[44:45] offset:2048 sc1\n"
    "s_cmp_lg_u32 s96, 2\n"
    "s_cbranch_scc1 NW%=\n"
    "s_waitcnt vmcnt(12)\n"
    "ds_read_b64_tr_b16 v[96:97], v228 offset:0\n"
    "ds_read_b64_tr_b16 v[98:99], v228 offset:256\n"
    "ds_read_b64_tr_b16 v[100:101], v228 offset:2048\n"
    "ds_read_b64_tr_b16 v[102:103], v228 offset:2304\n"
    "ds_read_b64_tr_b16 v[104:105], v228 offset:4096\n"
    "ds_read_b64_tr_b16 v[106:107], v228 offset:4352\n"
    "s_waitcnt lgkmcnt(0)\n"
    "v_mfma_f32_16x16x32_bf16 v[0:3], a[192:195], v[96:99], 0\n"
    "v_mfma_f32_16x16x32_bf16 v[4:7], a[204:207], v[96:99], 0\n"
    "v_mfma_f32_16x16x32_bf16 v[0:3], a[196:199], v[100:103], v[0:3]\n"
    "v_mfma_f32_16x16x32_bf16 v[4:7], a[208:211], v[100:103], v[4:7]\n"
    "v_mfma_f32_16x16x32_bf16 v[0:3], a[200:203], v[104:107], v[0:3]\n"
    "v_mfma_f32_16x16x32_bf16 v[4:7], a[212:215], v[104:107], v[4:7]\n"
    "s_nop 6\n"
    "v_cvt_pk_bf16_f32 v8, v0, v1\n"
    "v_cvt_pk_bf16_f32 v9, v2, v3\n"
    "s_nop 0\n"
    "twins_p1:\n"
    "s_cbranch_execz twins_p5\n"
    "v_cvt_pk_bf16_f32 v10, v4, v5\n"
    "v_cvt_pk_bf16_f32 v11, v6, v7\n"
    "global_store_dwordx4 v226, v[8:11], s[84:85] sc1\n"
    "s_waitcnt vmcnt(7)\n"
    "s_branch NX%=\n"
    "NW%=:\n"
    "s_waitcnt vmcnt(6)\n"
    "NX%=:\n"
    "s_barrier\n"
    "v_mfma_f32_16x16x32_bf16 v[0:3], a[96:99], a[180:183], 0\n"
    "ds_read_b128 a[48:51], v168 offset:0\n"
    "v_mov_b64 v[24:25], 0\n"
    "v_mfma_f32_16x16x32_bf16 v[0:3], a[100:103], a[184:187], v[0:3]\n"
    "ds_read_b128 a[52:55], v168 offset:64\n"
    "v_mov_b64 v[52:53], 0\n"
    "v_mfma_f32_16x16x32_bf16 v[0:3], a[104:107], a[188:191], v[0:3]\n"
    "ds_read_b128 a[56:59], v168 offset:128\n"
    "v_mov_b64 v[80:81], 0\n"
    "v_mfma_f32_16x16x32_bf16 v[4:7], a[108:111], a[180:183], 0\n"
    "ds_read_b128 a[60:63], v168 offset:3072\n"
    "v_mfma_f32_16x16x32_bf16 v[4:7], a[112:115], a[184:187], v[4:7]\n"
    "ds_read_b128 a[64:67], v168 offset:3136\n"
    "v_mfma_f32_16x16x32_bf16 v[4:7], a[116:119], a[188:191], v[4:7]\n"
    "ds_read_b128 a[68:71], v168 offset:3200\n"
    "v_mfma_f32_16x16x32_bf16 v[8:11], a[120:123], a[180:183], 0\n"
    "ds_read_b128 a[72:75], v171 offset:0\n"
    "v_cvt_pk_bf16_f32 v36, v0, v1\n"
    "v_mfma_f32_16x16x32_bf16 v[8:11], a[124:127], a[184:187], v[8:11]\n"
    "ds_read_b128 a[76:79], v171 offset:64\n"
    "v_cvt_pk_bf16_f32 v37, v2, v3\n"
    "v_mfma_f32_16x16x32_bf16 v[8:11], a[128:131], a[188:191], v[8:11]\n"
    "ds_read_b128 a[80:83], v171 offset:128\n"
    "v_cvt_pk_bf16_f32 v38, v4, v5\n"
    "v_mfma_f32_16x16x32_bf16 v[12:15], a[132:135], a[180:183], 0\n"
    "ds_read_b128 a[84:87], v171 offset:3072\n"
    "v_cvt_pk_bf16_f32 v39, v6, v7\n"
    "v_mfma_f32_16x16x32_bf16 v[12:15], a[136:139], a[184:187], v[12:15]\n"
    "ds_read_b128 a[88:91], v171 offset:3136\n"
    "v_accvgpr_write_b32 a36, v36\n"
    "v_mfma_f32_16x16x32_bf16 v[12:15], a[140:143], a[188:191], v[12:15]\n"
    "ds_read_b128 a[92:95], v171 offset:3200\n"
    "v_cvt_pk_bf16_f32 v32, v8, v9\n"
    "v_mfma_f32_16x16x32_bf16 v[16:19], a[216:219], a[180:183], 0\n"
    "ds_read_b64_tr_b16 a[144:145], v169 offset:0\n"
    "v_cvt_pk_bf16_f32 v33, v10, v11\n"
    "v_mfma_f32_16x16x32_bf16 v[16:19], a[220:223], a[184:187], v[16:19]\n"
    "ds_read_b64_tr_b16 a[146:147], v169 offset:3072\n"
    "v_accvgpr_write_b32 a37, v37\n"
    "v_mfma_f32_16x16x32_bf16 v[16:19], a[224:227], a[188:191], v[16:19]\n"
    "ds_read_b64_tr_b16 a[168:169], v172 offset:0\n"
    "v_cvt_pk_bf16_f32 v34, v12, v13\n"
    "v_mfma_f32_16x16x32_bf16 v[20:23], a[228:231], a[180:183], 0\n"
    "ds_read_b64_tr_b16 a[170:171], v172 offset:3072\n"
    "v_cvt_pk_bf16_f32 v35, v14, v15\n"
    "v_mfma_f32_16x16x32_bf16 v[20:23], a[232:235], a[184:187], v[20:23]\n"
    "ds_read_b64_tr_b16 a[148:149], v170 offset:0\n"
    "v_accvgpr_write_b32 a38, v38\n"
    "v_mfma_f32_16x16x32_bf16 v[20:23], a[236:239], a[188:191], v[20:23]\n"
    "s_waitcnt lgkmcnt(6)\n"
    "ds_read_b64_tr_b16 a[150:151], v170 offset:3072\n"
    "v_cvt_pk_bf16_f32 v36, v16, v17\n"
    "v_mfma_f32_16x16x32_bf16 v[112:115], a[48:51], a[0:3], 0\n"
    "ds_read_b64_tr_b16 a[172:173], v173 offset:0\n"
    "v_cvt_pk_bf16_f32 v37, v18, v19\n"
    "v_mfma_f32_16x16x32_bf16 v[112:115], a[52:55], a[4:7], v[112:115]\n"
    "ds_read_b64_tr_b16 a[174:175], v173 offset:3072\n"
    "v_accvgpr_write_b32 a39, v39\n"
    "v_mfma_f32_16x16x32_bf16 v[112:115], a[56:59], a[8:11], v[112:115]\n"
    "ds_read_b64_tr_b16 a[152:153], v169 offset:64\n"
    "v_cvt_pk_bf16_f32 v38, v20, v21\n"
    "v_mfma_f32_16x16x32_bf16 v[116:119], a[60:63], a[0:3], 0\n"
    "ds_read_b64_tr_b16 a[154:155], v169 offset:3136\n"
    "v_cvt_pk_bf16_f32 v39, v22, v23\n"
    "v_mfma_f32_16x16x32_bf16 v[116:119], a[64:67], a[4:7], v[116:119]\n"
    "ds_read_b64_tr_b16 a[176:177], v172 offset:64\n"
    "v_accvgpr_write_b32 a40, v32\n"
    "v_mfma_f32_16x16x32_bf16 v[116:119], a[68:71], a[8:11], v[116:119]\n"
    "ds_read_b64_tr_b16 a[178:179], v172 offset:3136\n"
    "v_accvgpr_write_b32 a41, v33\n"
    "v_mfma_f32_16x16x32_bf16 v[120:123], a[72:75], a[0:3], 0\n"
    "ds_read_b64_tr_b16 a[156:157], v170 offset:64\n"
    "v_accvgpr_write_b32 a42, v34\n"
    "v_mfma_f32_16x16x32_bf16 v[120:123], a[76:79], a[4:7], v[120:123]\n"
    "ds_read_b64_tr_b16 a[158:159], v170 offset:3136\n"
    "v_accvgpr_write_b32 a43, v35\n"
    "v_mfma_f32_16x16x32_bf16 v[120:123], a[80:83], a[8:11], v[120:123]\n"
    "ds_read_b64_tr_b16 a[160:161], v169 offset:128\n"
    "v_accvgpr_write_b32 a44, v36\n"
    "v_mfma_f32_16x16x32_bf16 v[124:127], a[84:87], a[0:3], 0\n"
    "ds_read_b64_tr_b16 a[162:163], v169 offset:3200\n"
    "v_accvgpr_write_b32 a45, v37\n"
    "v_mfma_f32_16x16x32_bf16 v[124:127], a[88:91], a[4:7], v[124:127]\n"
    "s_waitcnt lgkmcnt(6)\n"
    "ds_read_b64_tr_b16 a[164:165], v170 offset:128\n"
    "v_accvgpr_write_b32 a46, v38\n"
    "v_mfma_f32_16x16x32_bf16 v[124:127], a[92:95], a[8:11], v[124:127]\n"
    "ds_read_b64_tr_b16 a[166:167], v170 offset:3200\n"
    "v_accvgpr_write_b32 a47, v39\n"
    "s_nop 3\n"
    "ds_read_b64_tr_b16 a[180:181], v173 offset:64\n"
    "ds_read_b64_tr_b16 a[182:183], v173 offset:3136\n"
    "ds_read_b64_tr_b16 a[184:185], v172 offset:128\n"
    "ds_read_b64_tr_b16 a[186:187], v172 offset:3200\n"
    "ds_read_b64_tr_b16 a[188:189], v173 offset:128\n"
    "ds_read_b64_tr_b16 a[190:191], v173 offset:3200\n"
    "v_exp_f32 v112, v112\n"
    "v_exp_f32 v113, v113\n"
    "v_exp_f32 v114, v114\n"
    "v_exp_f32 v115, v115\n"
    "v_exp_f32 v116, v116\n"
    "v_exp_f32 v117, v117\n"
    "v_exp_f32 v118, v118\n"
    "v_exp_f32 v119, v119\n"
    "v_exp_f32 v120, v120\n"
    "v_exp_f32 v121, v121\n"
    "v_exp_f32 v122, v122\n"
    "v_exp_f32 v123, v123\n"
    "v_exp_f32 v124, v124\n"
    "v_exp_f32 v125, v125\n"
    "v_exp_f32 v126, v126\n"
    "v_exp_f32 v127, v127\n"
    "v_mfma_f32_16x16x32_bf16 v[128:131], a[48:51], a[12:15], 0\n"
    "ds_read_b128 a[96:99], v174 offset:0\n"
    "v_cvt_pk_bf16_f32 v152, v112, v113\n"
    "v_mfma_f32_16x16x32_bf16 v[128:131], a[52:55], a[16:19], v[128:131]\n"
    "ds_read_b128 a[100:103], v174 offset:64\n"
    "v_cvt_pk_bf16_f32 v153, v114, v115\n"
    "v_mfma_f32_16x16x32_bf16 v[128:131], a[56:59], a[20:23], v[128:131]\n"
    "ds_read_b128 a[104:107], v174 offset:128\n"
    "v_cvt_pk_bf16_f32 v154, v116, v117\n"
    "v_mfma_f32_16x16x32_bf16 v[132:135], a[60:63], a[12:15], 0\n"
    "ds_read_b128 a[108:111], v174 offset:3072\n"
    "v_cvt_pk_bf16_f32 v155, v118, v119\n"
    "v_mfma_f32_16x16x32_bf16 v[132:135], a[64:67], a[16:19], v[132:135]\n"
    "ds_read_b128 a[112:115], v174 offset:3136\n"
    "v_cvt_pk_bf16_f32 v156, v120, v121\n"
    "v_mfma_f32_16x16x32_bf16 v[132:135], a[68:71], a[20:23], v[132:135]\n"
    "ds_read_b128 a[116:119], v174 offset:3200\n"
    "v_cvt_pk_bf16_f32 v157, v122, v123\n"
    "v_mfma_f32_16x16x32_bf16 v[136:139], a[72:75], a[12:15], 0\n"
    "ds_read_b128 a[120:123], v177 offset:0\n"
    "v_cvt_pk_bf16_f32 v158, v124, v125\n"
    "v_mfma_f32_16x16x32_bf16 v[136:139], a[76:79], a[16:19], v[136:139]\n"
    "ds_read_b128 a[124:127], v177 offset:64\n"
    "v_cvt_pk_bf16_f32 v159, v126, v127\n"
    "v_mfma_f32_16x16x32_bf16 v[136:139], a[80:83], a[20:23], v[136:139]\n"
    "v_exp_f32 v128, v128\n"
    "v_mfma_f32_16x16x32_bf16 v[140:143], a[84:87], a[12:15], 0\n"
    "v_exp_f32 v129, v129\n"
    "v_mfma_f32_16x16x32_bf16 v[140:143], a[88:91], a[16:19], v[140:143]\n"
    "v_exp_f32 v130, v130\n"
    "v_mfma_f32_16x16x32_bf16 v[140:143], a[92:95], a[20:23], v[140:143]\n"
    "v_exp_f32 v131, v131\n"
    "v_smfmac_f32_16x16x64_bf16 v[24:27], a[240:243], v[152:159], v239\n"
    "ds_read_b128 a[128:131], v177 offset:128\n"
    "ds_read_b128 a[132:135], v177 offset:3072\n"
    "v_mfma_f32_16x16x32_bf16 v[0:3], a[144:147], v[152:155], 0\n"
    "v_exp_f32 v132, v132\n"
    "v_mfma_f32_16x16x32_bf16 v[0:3], a[168:171], v[156:159], v[0:3]\n"
    "v_exp_f32 v133, v133\n"
    "v_mfma_f32_16x16x32_bf16 v[4:7], a[148:151], v[152:155], 0\n"
    "v_exp_f32 v134, v134\n"
    "v_mfma_f32_16x16x32_bf16 v[4:7], a[172:175], v[156:159], v[4:7]\n"
    "ds_read_b128 a[136:139], v177 offset:3136\n"
    "v_exp_f32 v135, v135\n"
    "v_mfma_f32_16x16x32_bf16 v[8:11], a[152:155], v[152:155], 0\n"
    "s_waitcnt lgkmcnt(3)\n"
    "v_exp_f32 v136, v136\n"
    "v_mfma_f32_16x16x32_bf16 v[8:11], a[176:179], v[156:159], v[8:11]\n"
    "v_exp_f32 v137, v137\n"
    "v_mfma_f32_16x16x32_bf16 v[12:15], a[156:159], v[152:155], 0\n"
    "v_exp_f32 v138, v138\n"
    "v_mfma_f32_16x16x32_bf16 v[12:15], a[180:183], v[156:159], v[12:15]\n"
    "v_exp_f32 v139, v139\n"
    "v_mfma_f32_16x16x32_bf16 v[16:19], a[160:163], v[152:155], 0\n"
    "ds_read_b128 a[140:143], v177 offset:3200\n"
    "v_exp_f32 v140, v140\n"
    "v_mfma_f32_16x16x32_bf16 v[16:19], a[184:187], v[156:159], v[16:19]\n"
    "v_exp_f32 v141, v141\n"
    "v_mfma_f32_16x16x32_bf16 v[20:23], a[164:167], v[152:155], 0\n"
    "v_exp_f32 v142, v142\n"
    "v_mfma_f32_16x16x32_bf16 v[20:23], a[188:191], v[156:159], v[20:23]\n"
    "v_exp_f32 v143, v143\n"
    "v_mfma_f32_16x16x32_bf16 v[112:115], a[48:51], a[24:27], 0\n"
    "ds_read_b64_tr_b16 a[192:193], v175 offset:0\n"
    "v_cvt_pk_bf16_f32 v152, v128, v129\n"
    "v_mfma_f32_16x16x32_bf16 v[112:115], a[52:55], a[28:31], v[112:115]\n"
    "ds_read_b64_tr_b16 a[194:195], v175 offset:3072\n"
    "v_cvt_pk_bf16_f32 v153, v130, v131\n"
    "v_mfma_f32_16x16x32_bf16 v[112:115], a[56:59], a[32:35], v[112:115]\n"
    "ds_read_b64_tr_b16 a[216:217], v178 offset:0\n"
    "v_cvt_pk_bf16_f32 v154, v132, v133\n"
    "v_mfma_f32_16x16x32_bf16 v[116:119], a[60:63], a[24:27], 0\n"
    "ds_read_b64_tr_b16 a[218:219], v178 offset:3072\n"
    "v_cvt_pk_bf16_f32 v155, v134, v135\n"
    "v_mfma_f32_16x16x32_bf16 v[116:119], a[64:67], a[28:31], v[116:119]\n"
    "ds_read_b64_tr_b16 a[196:197], v176 offset:0\n"
    "v_cvt_pk_bf16_f32 v156, v136, v137\n"
    "v_mfma_f32_16x16x32_bf16 v[116:119], a[68:71], a[32:35], v[116:119]\n"
    "ds_read_b64_tr_b16 a[198:199], v176 offset:3072\n"
    "v_cvt_pk_bf16_f32 v157, v138, v139\n"
    "v_mfma_f32_16x16x32_bf16 v[120:123], a[72:75], a[24:27], 0\n"
    "ds_read_b64_tr_b16 a[220:221], v179 offset:0\n"
    "v_cvt_pk_bf16_f32 v158, v140, v141\n"
    "v_mfma_f32_16x16x32_bf16 v[120:123], a[76:79], a[28:31], v[120:123]\n"
    "ds_read_b64_tr_b16 a[222:223], v179 offset:3072\n"
    "v_cvt_pk_bf16_f32 v159, v142, v143\n"
    "v_mfma_f32_16x16x32_bf16 v[120:123], a[80:83], a[32:35], v[120:123]\n"
    "v_exp_f32 v112, v112\n"
    "v_mfma_f32_16x16x32_bf16 v[124:127], a[84:87], a[24:27], 0\n"
    "v_exp_f32 v113, v113\n"
    "v_mfma_f32_16x16x32_bf16 v[124:127], a[88:91], a[28:31], v[124:127]\n"
    "v_exp_f32 v114, v114\n"
    "v_mfma_f32_16x16x32_bf16 v[124:127], a[92:95], a[32:35], v[124:127]\n"
    "v_exp_f32 v115, v115\n"
    "v_smfmac_f32_16x16x64_bf16 v[52:55], a[240:243], v[152:159], v239\n"
    "ds_read_b64_tr_b16 a[200:201], v175 offset:64\n"
    "ds_read_b64_tr_b16 a[202:203], v175 offset:3136\n"
    "v_mfma_f32_16x16x32_bf16 v[28:31], a[144:147], v[152:155], 0\n"
    "v_exp_f32 v116, v116\n"
    "v_mfma_f32_16x16x32_bf16 v[28:31], a[168:171], v[156:159], v[28:31]\n"
    "v_exp_f32 v117, v117\n"
    "v_mfma_f32_16x16x32_bf16 v[32:35], a[148:151], v[152:155], 0\n"
    "v_exp_f32 v118, v118\n"
    "v_mfma_f32_16x16x32_bf16 v[32:35], a[172:175], v[156:159], v[32:35]\n"
    "ds_read_b64_tr_b16 a[224:225], v178 offset:64\n"
    "v_exp_f32 v119, v119\n"
    "v_mfma_f32_16x16x32_bf16 v[36:39], a[152:155], v[152:155], 0\n"
    "v_exp_f32 v120, v120\n"
    "v_mfma_f32_16x16x32_bf16 v[36:39], a[176:179], v[156:159], v[36:39]\n"
    "v_exp_f32 v121, v121\n"
    "v_mfma_f32_16x16x32_bf16 v[40:43], a[156:159], v[152:155], 0\n"
    "v_exp_f32 v122, v122\n"
    "v_mfma_f32_16x16x32_bf16 v[40:43], a[180:183], v[156:159], v[40:43]\n"
    "v_exp_f32 v123, v123\n"
    "v_mfma_f32_16x16x32_bf16 v[44:47], a[160:163], v[152:155], 0\n"
    "ds_read_b64_tr_b16 a[226:227], v178 offset:3136\n"
    "v_exp_f32 v124, v124\n"
    "v_mfma_f32_16x16x32_bf16 v[44:47], a[184:187], v[156:159], v[44:47]\n"
    "v_exp_f32 v125, v125\n"
    "v_mfma_f32_16x16x32_bf16 v[48:51], a[164:167], v[152:155], 0\n"
    "v_exp_f32 v126, v126\n"
    "v_mfma_f32_16x16x32_bf16 v[48:51], a[188:191], v[156:159], v[48:51]\n"
    "v_exp_f32 v127, v127\n"
    "v_mfma_f32_16x16x32_bf16 v[128:131], a[96:99], a[0:3], 0\n"
    "ds_read_b64_tr_b16 a[204:205], v176 offset:64\n"
    "v_cvt_pk_bf16_f32 v152, v112, v113\n"
    "v_mfma_f32_16x16x32_bf16 v[128:131], a[100:103], a[4:7], v[128:131]\n"
    "ds_read_b64_tr_b16 a[206:207], v176 offset:3136\n"
    "v_cvt_pk_bf16_f32 v153, v114, v115\n"
    "v_mfma_f32_16x16x32_bf16 v[128:131], a[104:107], a[8:11], v[128:131]\n"
    "ds_read_b64_tr_b16 a[228:229], v179 offset:64\n"
    "v_cvt_pk_bf16_f32 v154, v116, v117\n"
    "v_mfma_f32_16x16x32_bf16 v[132:135], a[108:111], a[0:3], 0\n"
    "ds_read_b64_tr_b16 a[230:231], v179 offset:3136\n"
    "v_cvt_pk_bf16_f32 v155, v118, v119\n"
    "v_mfma_f32_16x16x32_bf16 v[132:135], a[112:115], a[4:7], v[132:135]\n"
    "ds_read_b64_tr_b16 a[208:209], v175 offset:128\n"
    "v_cvt_pk_bf16_f32 v156, v120, v121\n"
    "v_mfma_f32_16x16x32_bf16 v[132:135], a[116:119], a[8:11], v[132:135]\n"
    "ds_read_b64_tr_b16 a[210:211], v175 offset:3200\n"
    "v_cvt_pk_bf16_f32 v157, v122, v123\n"
    "v_mfma_f32_16x16x32_bf16 v[136:139], a[120:123], a[0:3], 0\n"
    "ds_read_b64_tr_b16 a[232:233], v178 offset:128\n"
    "v_cvt_pk_bf16_f32 v158, v124, v125\n"
    "v_mfma_f32_16x16x32_bf16 v[136:139], a[124:127], a[4:7], v[136:139]\n"
    "s_waitcnt lgkmcnt(6)\n"
    "ds_read_b64_tr_b16 a[234:235], v178 offset:3200\n"
    "v_cvt_pk_bf16_f32 v159, v126, v127\n"
    "v_mfma_f32_16x16x32_bf16 v[136:139], a[128:131], a[8:11], v[136:139]\n"
    "v_exp_f32 v128, v128\n"
    "v_mfma_f32_16x16x32_bf16 v[140:143], a[132:135], a[0:3], 0\n"
    "v_exp_f32 v129, v129\n"
    "v_mfma_f32_16x16x32_bf16 v[140:143], a[136:139], a[4:7], v[140:143]\n"
    "v_exp_f32 v130, v130\n"
    "v_mfma_f32_16x16x32_bf16 v[140:143], a[140:143], a[8:11], v[140:143]\n"
    "v_exp_f32 v131, v131\n"
    "v_smfmac_f32_16x16x64_bf16 v[80:83], a[240:243], v[152:159], v239\n"
    "ds_read_b64_tr_b16 a[212:213], v176 offset:128\n"
    "ds_read_b64_tr_b16 a[214:215], v176 offset:3200\n"
    "v_mfma_f32_16x16x32_bf16 v[56:59], a[144:147], v[152:155], 0\n"
    "v_exp_f32 v132, v132\n"
    "v_mfma_f32_16x16x32_bf16 v[56:59], a[168:171], v[156:159], v[56:59]\n"
    "v_exp_f32 v133, v133\n"
    "v_mfma_f32_16x16x32_bf16 v[60:63], a[148:151], v[152:155], 0\n"
    "v_exp_f32 v134, v134\n"
    "v_mfma_f32_16x16x32_bf16 v[60:63], a[172:175], v[156:159], v[60:63]\n"
    "ds_read_b64_tr_b16 a[236:237], v179 offset:128\n"
    "v_exp_f32 v135, v135\n"
    "v_mfma_f32_16x16x32_bf16 v[64:67], a[152:155], v[152:155], 0\n"
    "v_exp_f32 v136, v136\n"
    "v_mfma_f32_16x16x32_bf16 v[64:67], a[176:179], v[156:159], v[64:67]\n"
    "v_exp_f32 v137, v137\n"
    "v_mfma_f32_16x16x32_bf16 v[68:71], a[156:159], v[152:155], 0\n"
    "v_exp_f32 v138, v138\n"
    "v_mfma_f32_16x16x32_bf16 v[68:71], a[180:183], v[156:159], v[68:71]\n"
    "v_exp_f32 v139, v139\n"
    "v_mfma_f32_16x16x32_bf16 v[72:75], a[160:163], v[152:155], 0\n"
    "ds_read_b64_tr_b16 a[238:239], v179 offset:3200\n"
    "v_exp_f32 v140, v140\n"
    "v_mfma_f32_16x16x32_bf16 v[72:75], a[184:187], v[156:159], v[72:75]\n"
    "v_exp_f32 v141, v141\n"
    "v_mfma_f32_16x16x32_bf16 v[76:79], a[164:167], v[152:155], 0\n"
    "v_exp_f32 v142, v142\n"
    "v_mfma_f32_16x16x32_bf16 v[76:79], a[188:191], v[156:159], v[76:79]\n"
    "v_exp_f32 v143, v143\n"
    "v_mfma_f32_16x16x32_bf16 v[112:115], a[96:99], a[12:15], 0\n"
    "s_waitcnt vmcnt(0)\n"
    "v_cvt_pk_bf16_f32 v152, v128, v129\n"
    "v_cvt_pk_bf16_f32 v153, v130, v131\n"
    "v_mfma_f32_16x16x32_bf16 v[112:115], a[100:103], a[16:19], v[112:115]\n"
    "s_barrier\n"
    "v_cvt_pk_bf16_f32 v154, v132, v133\n"
    "v_cvt_pk_bf16_f32 v155, v134, v135\n"
    "v_mfma_f32_16x16x32_bf16 v[112:115], a[104:107], a[20:23], v[112:115]\n"
    "ds_read_b128 a[48:51], v168 offset:24576\n"
    "v_cvt_pk_bf16_f32 v156, v136, v137\n"
    "v_mfma_f32_16x16x32_bf16 v[116:119], a[108:111], a[12:15], 0\n"
    "ds_read_b128 a[52:55], v168 offset:24640\n"
    "v_cvt_pk_bf16_f32 v157, v138, v139\n"
    "v_mfma_f32_16x16x32_bf16 v[116:119], a[112:115], a[16:19], v[116:119]\n"
    "ds_read_b128 a[56:59], v168 offset:24704\n"
    "v_cvt_pk_bf16_f32 v158, v140, v141\n"
    "v_mfma_f32_16x16x32_bf16 v[116:119], a[116:119], a[20:23], v[116:119]\n"
    "ds_read_b128 a[60:63], v168 offset:27648\n"
    "v_cvt_pk_bf16_f32 v159, v142, v143\n"
    "v_mfma_f32_16x16x32_bf16 v[120:123], a[120:123], a[12:15], 0\n"
    "v_exp_f32 v112, v112\n"
    "v_mfma_f32_16x16x32_bf16 v[120:123], a[124:127], a[16:19], v[120:123]\n"
    "v_exp_f32 v113, v113\n"
    "v_mfma_f32_16x16x32_bf16 v[120:123], a[128:131], a[20:23], v[120:123]\n"
    "ds_read_b128 a[64:67], v168 offset:27712\n"
    "ds_read_b128 a[68:71], v168 offset:27776\n"
    "v_mfma_f32_16x16x32_bf16 v[124:127], a[132:135], a[12:15], 0\n"
    "global_load_dwordx2 v[224:225], v231, s[90:91] sc1\n"
    "v_exp_f32 v114, v114\n"
    "v_mfma_f32_16x16x32_bf16 v[124:127], a[136:139], a[16:19], v[124:127]\n"
    "v_exp_f32 v115, v115\n"
    "v_mfma_f32_16x16x32_bf16 v[124:127], a[140:143], a[20:23], v[124:127]\n"
    "v_exp_f32 v116, v116\n"
    "v_smfmac_f32_16x16x64_bf16 v[24:27], a[240:243], v[152:159], v239\n"
    "ds_read_b128 a[72:75], v171 offset:24576\n"
    "ds_read_b128 a[76:79], v171 offset:24640\n"
    "v_mfma_f32_16x16x32_bf16 v[0:3], a[192:195], v[152:155], v[0:3]\n"
    "v_exp_f32 v117, v117\n"
    "v_mfma_f32_16x16x32_bf16 v[0:3], a[216:219], v[156:159], v[0:3]\n"
    "v_exp_f32 v118, v118\n"
    "v_mfma_f32_16x16x32_bf16 v[4:7], a[196:199], v[152:155], v[4:7]\n"
    "v_exp_f32 v119, v119\n"
    "v_mfma_f32_16x16x32_bf16 v[4:7], a[220:223], v[156:159], v[4:7]\n"
    "v_exp_f32 v120, v120\n"
    "v_mfma_f32_16x16x32_bf16 v[8:11], a[200:203], v[152:155], v[8:11]\n"
    "ds_read_b128 a[80:83], v171 offset:24704\n"
    "ds_read_b128 a[84:87], v171 offset:27648\n"
    "v_mfma_f32_16x16x32_bf16 v[8:11], a[224:227], v[156:159], v[8:11]\n"
    "s_waitcnt lgkmcnt(4)\n"
    "v_exp_f32 v121, v121\n"
    "v_mfma_f32_16x16x32_bf16 v[12:15], a[204:207], v[152:155], v[12:15]\n"
    "v_exp_f32 v122, v122\n"
    "v_mfma_f32_16x16x32_bf16 v[12:15], a[228:231], v[156:159], v[12:15]\n"
    "ds_read_b128 a[88:91], v171 offset:27712\n"
    "v_exp_f32 v123, v123\n"
    "v_mfma_f32_16x16x32_bf16 v[16:19], a[208:211], v[152:155], v[16:19]\n"
    "v_exp_f32 v124, v124\n"
    "v_mfma_f32_16x16x32_bf16 v[16:19], a[232:235], v[156:159], v[16:19]\n"
    "v_exp_f32 v125, v125\n"
    "v_mfma_f32_16x16x32_bf16 v[20:23], a[212:215], v[152:155], v[20:23]\n"
    "ds_read_b128 a[92:95], v171 offset:27776\n"
    "v_exp_f32 v126, v126\n"
    "v_mfma_f32_16x16x32_bf16 v[20:23], a[236:239], v[156:159], v[20:23]\n"
    "v_exp_f32 v127, v127\n"
    "v_mfma_f32_16x16x32_bf16 v[128:131], a[96:99], a[24:27], 0\n"
    "ds_read_b64_tr_b16 a[144:145], v169 offset:24576\n"
    "v_cvt_pk_bf16_f32 v152, v112, v113\n"
    "v_mfma_f32_16x16x32_bf16 v[128:131], a[100:103], a[28:31], v[128:131]\n"
    "ds_read_b64_tr_b16 a[146:147], v169 offset:27648\n"
    "v_cvt_pk_bf16_f32 v153, v114, v115\n"
    "v_mfma_f32_16x16x32_bf16 v[128:131], a[104:107], a[32:35], v[128:131]\n"
    "ds_read_b64_tr_b16 a[168:169], v172 offset:24576\n"
    "v_cvt_pk_bf16_f32 v154, v116, v117\n"
    "v_mfma_f32_16x16x32_bf16 v[132:135], a[108:111], a[24:27], 0\n"
    "ds_read_b64_tr_b16 a[170:171], v172 offset:27648\n"
    "v_cvt_pk_bf16_f32 v155, v118, v119\n"
    "v_mfma_f32_16x16x32_bf16 v[132:135], a[112:115], a[28:31], v[132:135]\n"
    "ds_read_b64_tr_b16 a[148:149], v170 offset:24576\n"
    "v_cvt_pk_bf16_f32 v156, v120, v121\n"
    "v_mfma_f32_16x16x32_bf16 v[132:135], a[116:119], a[32:35], v[132:135]\n"
    "ds_read_b64_tr_b16 a[150:151], v170 offset:27648\n"
    "v_cvt_pk_bf16_f32 v157, v122, v123\n"
    "v_mfma_f32_16x16x32_bf16 v[136:139], a[120:123], a[24:27], 0\n"
    "ds_read_b64_tr_b16 a[172:173], v173 offset:24576\n"
    "v_cvt_pk_bf16_f32 v158, v124, v125\n"
    "v_mfma_f32_16x16x32_bf16 v[136:139], a[124:127], a[28:31], v[136:139]\n"
    "ds_read_b64_tr_b16 a[174:175], v173 offset:27648\n"
    "v_cvt_pk_bf16_f32 v159, v126, v127\n"
    "v_mfma_f32_16x16x32_bf16 v[136:139], a[128:131], a[32:35], v[136:139]\n"
    "v_exp_f32 v128, v128\n"
    "v_mfma_f32_16x16x32_bf16 v[140:143], a[132:135], a[24:27], 0\n"
    "v_exp_f32 v129, v129\n"
    "v_mfma_f32_16x16x32_bf16 v[140:143], a[136:139], a[28:31], v[140:143]\n"
    "v_exp_f32 v130, v130\n"
    "v_mfma_f32_16x16x32_bf16 v[140:143], a[140:143], a[32:35], v[140:143]\n"
    "v_exp_f32 v131, v131\n"
    "v_mfma_f32_16x16x32_bf16 v[144:147], a[120:123], a[36:39], v[164:167]\n"
    "v_exp_f32 v132, v132\n"
    "v_mfma_f32_16x16x32_bf16 v[144:147], a[124:127], a[40:43], v[144:147]\n"
    "v_exp_f32 v133, v133\n"
    "v_mfma_f32_16x16x32_bf16 v[144:147], a[128:131], a[44:47], v[144:147]\n"
    "v_exp_f32 v134, v134\n"
    "v_mfma_f32_16x16x32_bf16 v[148:151], a[132:135], a[36:39], v[164:167]\n"
    "v_exp_f32 v135, v135\n"
    "v_mfma_f32_16x16x32_bf16 v[148:151], a[136:139], a[40:43], v[148:151]\n"
    "v_exp_f32 v136, v136\n"
    "v_mfma_f32_16x16x32_bf16 v[148:151], a[140:143], a[44:47], v[148:151]\n"
    "v_exp_f32 v137, v137\n"
    "v_smfmac_f32_16x16x64_bf16 v[52:55], a[240:243], v[152:159], v239\n"
    "ds_read_b64_tr_b16 a[152:153], v169 offset:24640\n"
    "ds_read_b64_tr_b16 a[154:155], v169 offset:27712\n"
    "v_mfma_f32_16x16x32_bf16 v[28:31], a[192:195], v[152:155], v[28:31]\n"
    "v_exp_f32 v138, v138\n"
    "v_mfma_f32_16x16x32_bf16 v[28:31], a[216:219], v[156:159], v[28:31]\n"
    "v_exp_f32 v139, v139\n"
    "v_mfma_f32_16x16x32_bf16 v[32:35], a[196:199], v[152:155], v[32:35]\n"
    "v_exp_f32 v140, v140\n"
    "v_mfma_f32_16x16x32_bf16 v[32:35], a[220:223], v[156:159], v[32:35]\n"
    "v_exp_f32 v141, v141\n"
    "v_mfma_f32_16x16x32_bf16 v[36:39], a[200:203], v[152:155], v[36:39]\n"
    "v_exp_f32 v142, v142\n"
    "v_mfma_f32_16x16x32_bf16 v[36:39], a[224:227], v[156:159], v[36:39]\n"
    "v_exp_f32 v143, v143\n"
    "v_mfma_f32_16x16x32_bf16 v[40:43], a[204:207], v[152:155], v[40:43]\n"
    "v_exp_f32 v144, v144\n"
    "v_mfma_f32_16x16x32_bf16 v[40:43], a[228:231], v[156:159], v[40:43]\n"
    "v_exp_f32 v145, v145\n"
    "v_mfma_f32_16x16x32_bf16 v[44:47], a[208:211], v[152:155], v[44:47]\n"
    "v_exp_f32 v146, v146\n"
    "v_mfma_f32_16x16x32_bf16 v[44:47], a[232:235], v[156:159], v[44:47]\n"
    "s_waitcnt vmcnt(0)\n"
    "v_exp_f32 v147, v147\n"
    "v_cmp_ne_u64 vcc, s[58:59], v[224:225]\n"
    "v_mfma_f32_16x16x32_bf16 v[48:51], a[212:215], v[152:155], v[48:51]\n"
    "s_and_b32 s38, vcc_lo, 0xffff0000\n"
    "v_exp_f32 v148, v148\n"
    "v_mfma_f32_16x16x32_bf16 v[48:51], a[236:239], v[156:159], v[48:51]\n"
    "s_cbranch_scc1 Rc%=\n"
    "Bc%=:\n"
    "v_exp_f32 v149, v149\n"
    "v_mfma_f32_16x16x32_bf16 v[112:115], a[48:51], a[0:3], 0\n"
    "ds_read_b64_tr_b16 a[176:177], v172 offset:24640\n"
    "v_cvt_pk_bf16_f32 v152, v128, v129\n"
    "v_mfma_f32_16x16x32_bf16 v[112:115], a[52:55], a[4:7], v[112:115]\n"
    "ds_read_b64_tr_b16 a[178:179], v172 offset:27712\n"
    "v_cvt_pk_bf16_f32 v153, v130, v131\n"
    "v_mfma_f32_16x16x32_bf16 v[112:115], a[56:59], a[8:11], v[112:115]\n"
    "ds_read_b64_tr_b16 a[156:157], v170 offset:24640\n"
    "v_cvt_pk_bf16_f32 v154, v132, v133\n"
    "v_mfma_f32_16x16x32_bf16 v[116:119], a[60:63], a[0:3], 0\n"
    "ds_read_b64_tr_b16 a[158:159], v170 offset:27712\n"
    "v_cvt_pk_bf16_f32 v155, v134, v135\n"
    "v_mfma_f32_16x16x32_bf16 v[116:119], a[64:67], a[4:7], v[116:119]\n"
    "ds_read_b64_tr_b16 a[180:181], v173 offset:24640\n"
    "v_cvt_pk_bf16_f32 v156, v136, v137\n"
    "v_mfma_f32_16x16x32_bf16 v[116:119], a[68:71], a[8:11], v[116:119]\n"
    "s_waitcnt lgkmcnt(5)\n"
    "ds_read_b64_tr_b16 a[182:183], v173 offset:27712\n"
    "v_cvt_pk_bf16_f32 v157, v138, v139\n"
    "v_mfma_f32_16x16x32_bf16 v[120:123], a[72:75], a[0:3], 0\n"
    "ds_read_b64_tr_b16 a[160:161], v169 offset:24704\n"
    "v_cvt_pk_bf16_f32 v158, v140, v141\n"
    "v_mfma_f32_16x16x32_bf16 v[120:123], a[76:79], a[4:7], v[120:123]\n"
    "ds_read_b64_tr_b16 a[162:163], v169 offset:27776\n"
    "v_cvt_pk_bf16_f32 v159, v142, v143\n"
    "v_mfma_f32_16x16x32_bf16 v[120:123], a[80:83], a[8:11], v[120:123]\n"
    "v_exp_f32 v150, v150\n"
    "v_mfma_f32_16x16x32_bf16 v[124:127], a[84:87], a[0:3], 0\n"
    "v_exp_f32 v151, v151\n"
    "v_mfma_f32_16x16x32_bf16 v[124:127], a[88:91], a[4:7], v[124:127]\n"
    "ds_read_b64_tr_b16 a[184:185], v172 offset:24704\n"
    "v_cvt_pk_bf16_f32 v160, v144, v145\n"
    "v_mfma_f32_16x16x32_bf16 v[124:127], a[92:95], a[8:11], v[124:127]\n"
    "ds_read_b64_tr_b16 a[186:187], v172 offset:27776\n"
    "v_cvt_pk_bf16_f32 v161, v146, v147\n"
    "v_smfmac_f32_16x16x64_bf16 v[80:83], a[240:243], v[152:159], v239\n"
    "ds_read_b64_tr_b16 a[164:165], v170 offset:24704\n"
    "v_cvt_pk_bf16_f32 v162, v148, v149\n"
    "v_mfma_f32_16x16x32_bf16 v[56:59], a[192:195], v[152:155], v[56:59]\n"
    "ds_read_b64_tr_b16 a[166:167], v170 offset:27776\n"
    "v_cvt_pk_bf16_f32 v163, v150, v151\n"
    "v_mfma_f32_16x16x32_bf16 v[56:59], a[216:219], v[156:159], v[56:59]\n"
    "s_add_u32 m0, s40, 52224\n"
    "v_exp_f32 v112, v112\n"
    "v_mfma_f32_16x16x32_bf16 v[60:63], a[196:199], v[152:155], v[60:63]\n"
    "global_load_lds_dwordx4 v216, s[46:47] offset:-3072 sc1\n"
    "v_exp_f32 v113, v113\n"
    "v_mfma_f32_16x16x32_bf16 v[60:63], a[220:223], v[156:159], v[60:63]\n"
    "v_exp_f32 v114, v114\n"
    "v_mfma_f32_16x16x32_bf16 v[64:67], a[200:203], v[152:155], v[64:67]\n"
    "ds_read_b64_tr_b16 a[188:189], v173 offset:24704\n"
    "global_load_lds_dwordx4 v216, s[46:47] offset:-2048 sc1\n"
    "v_mfma_f32_16x16x32_bf16 v[64:67], a[224:227], v[156:159], v[64:67]\n"
    "v_exp_f32 v115, v115\n"
    "v_mfma_f32_16x16x32_bf16 v[68:71], a[204:207], v[152:155], v[68:71]\n"
    "v_exp_f32 v116, v116\n"
    "v_mfma_f32_16x16x32_bf16 v[68:71], a[228:231], v[156:159], v[68:71]\n"
    "global_load_lds_dwordx4 v216, s[46:47] offset:-1024 sc1\n"
    "v_exp_f32 v117, v117\n"
    "v_mfma_f32_16x16x32_bf16 v[72:75], a[208:211], v[152:155], v[72:75]\n"
    "v_exp_f32 v118, v118\n"
    "v_mfma_f32_16x16x32_bf16 v[72:75], a[232:235], v[156:159], v[72:75]\n"
    "v_exp_f32 v119, v119\n"
    "v_mfma_f32_16x16x32_bf16 v[76:79], a[212:215], v[152:155], v[76:79]\n"
    "ds_read_b64_tr_b16 a[190:191], v173 offset:27776\n"
    "global_load_lds_dwordx4 v216, s[46:47] offset:0 sc1\n"
    "v_mfma_f32_16x16x32_bf16 v[76:79], a[236:239], v[156:159], v[76:79]\n"
    "v_exp_f32 v120, v120\n"
    "v_mfma_f32_16x16x32_bf16 v[84:87], a[216:219], v[160:163], 0\n"
    "v_exp_f32 v121, v121\n"
    "v_mfma_f32_16x16x32_bf16 v[88:91], a[220:223], v[160:163], 0\n"
    "global_load_lds_dwordx4 v216, s[46:47] offset:1024 sc1\n"
    "v_exp_f32 v122, v122\n"
    "v_mfma_f32_16x16x32_bf16 v[92:95], a[224:227], v[160:163], 0\n"
    "v_exp_f32 v123, v123\n"
    "v_mfma_f32_16x16x32_bf16 v[96:99], a[228:231], v[160:163], 0\n"
    "v_exp_f32 v124, v124\n"
    "v_mfma_f32_16x16x32_bf16 v[100:103], a[232:235], v[160:163], 0\n"
    "global_load_lds_dwordx4 v216, s[46:47] offset:2048 sc1\n"
    "v_exp_f32 v125, v125\n"
    "v_mfma_f32_16x16x32_bf16 v[104:107], a[236:239], v[160:163], 0\n"
    "v_exp_f32 v126, v126\n"
    "v_mfma_f32_16x16x32_bf16 v[108:111], a[240:243], v[160:163], 0\n"
    "v_exp_f32 v127, v127\n"
    "v_mfma_f32_16x16x32_bf16 v[128:131], a[48:51], a[12:15], 0\n"
    "ds_read_b128 a[96:99], v174 offset:24576\n"
    "v_cvt_pk_bf16_f32 v152, v112, v113\n"
    "v_mfma_f32_16x16x32_bf16 v[128:131], a[52:55], a[16:19], v[128:131]\n"
    "ds_read_b128 a[100:103], v174 offset:24640\n"
    "v_cvt_pk_bf16_f32 v153, v114, v115\n"
    "v_mfma_f32_16x16x32_bf16 v[128:131], a[56:59], a[20:23], v[128:131]\n"
    "ds_read_b128 a[104:107], v174 offset:24704\n"
    "v_cvt_pk_bf16_f32 v154, v116, v117\n"
    "v_mfma_f32_16x16x32_bf16 v[132:135], a[60:63], a[12:15], 0\n"
    "ds_read_b128 a[108:111], v174 offset:27648\n"
    "v_cvt_pk_bf16_f32 v155, v118, v119\n"
    "v_mfma_f32_16x16x32_bf16 v[132:135], a[64:67], a[16:19], v[132:135]\n"
    "ds_read_b128 a[112:115], v174 offset:27712\n"
    "v_cvt_pk_bf16_f32 v156, v120, v121\n"
    "v_mfma_f32_16x16x32_bf16 v[132:135], a[68:71], a[20:23], v[132:135]\n"
    "ds_read_b128 a[116:119], v174 offset:27776\n"
    "v_cvt_pk_bf16_f32 v157, v122, v123\n"
    "v_mfma_f32_16x16x32_bf16 v[136:139], a[72:75], a[12:15], 0\n"
    "ds_read_b128 a[120:123], v177 offset:24576\n"
    "v_cvt_pk_bf16_f32 v158, v124, v125\n"
    "v_mfma_f32_16x16x32_bf16 v[136:139], a[76:79], a[16:19], v[136:139]\n"
    "ds_read_b128 a[124:127], v177 offset:24640\n"
    "v_cvt_pk_bf16_f32 v159, v126, v127\n"
    "v_mfma_f32_16x16x32_bf16 v[136:139], a[80:83], a[20:23], v[136:139]\n"
    "s_add_u32 m0, s40, 76800\n"
    "v_exp_f32 v128, v128\n"
    "v_mfma_f32_16x16x32_bf16 v[140:143], a[84:87], a[12:15], 0\n"
    "global_load_lds_dwordx4 v216, s[48:49] offset:-3072 sc1\n"
    "v_exp_f32 v129, v129\n"
    "v_mfma_f32_16x16x32_bf16 v[140:143], a[88:91], a[16:19], v[140:143]\n"
    "v_exp_f32 v130, v130\n"
    "v_mfma_f32_16x16x32_bf16 v[140:143], a[92:95], a[20:23], v[140:143]\n"
    "v_exp_f32 v131, v131\n"
    "v_smfmac_f32_16x16x64_bf16 v[24:27], a[240:243], v[152:159], v239\n"
    "ds_read_b128 a[128:131], v177 offset:24704\n"
    "ds_read_b128 a[132:135], v177 offset:27648\n"
    "v_mfma_f32_16x16x32_bf16 v[0:3], a[144:147], v[152:155], v[0:3]\n"
    "global_load_lds_dwordx4 v216, s[48:49] offset:-2048 sc1\n"
    "v_exp_f32 v132, v132\n"
    "v_mfma_f32_16x16x32_bf16 v[0:3], a[168:171], v[156:159], v[0:3]\n"
    "v_exp_f32 v133, v133\n"
    "v_mfma_f32_16x16x32_bf16 v[4:7], a[148:151], v[152:155], v[4:7]\n"
    "s_waitcnt lgkmcnt(2)\n"
    "v_exp_f32 v134, v134\n"
    "v_mfma_f32_16x16x32_bf16 v[4:7], a[172:175], v[156:159], v[4:7]\n"
    "ds_read_b128 a[136:139], v177 offset:27712\n"
    "v_exp_f32 v135, v135\n"
    "v_mfma_f32_16x16x32_bf16 v[8:11], a[152:155], v[152:155], v[8:11]\n"
    "global_load_lds_dwordx4 v216, s[48:49] offset:-1024 sc1\n"
    "v_exp_f32 v136, v136\n"
    "v_mfma_f32_16x16x32_bf16 v[8:11], a[176:179], v[156:159], v[8:11]\n"
    "v_exp_f32 v137, v137\n"
    "v_mfma_f32_16x16x32_bf16 v[12:15], a[156:159], v[152:155], v[12:15]\n"
    "v_exp_f32 v138, v138\n"
    "v_mfma_f32_16x16x32_bf16 v[12:15], a[180:183], v[156:159], v[12:15]\n"
    "v_exp_f32 v139, v139\n"
    "v_mfma_f32_16x16x32_bf16 v[16:19], a[160:163], v[152:155], v[16:19]\n"
    "ds_read_b128 a[140:143], v177 offset:27776\n"
    "v_exp_f32 v140, v140\n"
    "twins_p2:\n"
    "s_cbranch_execz twins_p6\n"
    "v_mfma_f32_16x16x32_bf16 v[16:19], a[184:187], v[156:159], v[16:19]\n"
    "v_exp_f32 v141, v141\n"
    "v_mfma_f32_16x16x32_bf16 v[20:23], a[164:167], v[152:155], v[20:23]\n"
    "v_exp_f32 v142, v142\n"
    "v_mfma_f32_16x16x32_bf16 v[20:23], a[188:191], v[156:159], v[20:23]\n"
    "v_exp_f32 v143, v143\n"
    "v_mfma_f32_16x16x32_bf16 v[112:115], a[48:51], a[24:27], 0\n"
    "ds_read_b64_tr_b16 a[192:193], v175 offset:24576\n"
    "v_cvt_pk_bf16_f32 v152, v128, v129\n"
    "v_mfma_f32_16x16x32_bf16 v[112:115], a[52:55], a[28:31], v[112:115]\n"
    "ds_read_b64_tr_b16 a[194:195], v175 offset:27648\n"
    "v_cvt_pk_bf16_f32 v153, v130, v131\n"
    "v_mfma_f32_16x16x32_bf16 v[112:115], a[56:59], a[32:35], v[112:115]\n"
    "ds_read_b64_tr_b16 a[216:217], v178 offset:24576\n"
    "v_cvt_pk_bf16_f32 v154, v132, v133\n"
    "v_mfma_f32_16x16x32_bf16 v[116:119], a[60:63], a[24:27], 0\n"
    "ds_read_b64_tr_b16 a[218:219], v178 offset:27648\n"
    "v_cvt_pk_bf16_f32 v155, v134, v135\n"
    "v_mfma_f32_16x16x32_bf16 v[116:119], a[64:67], a[28:31], v[116:119]\n"
    "ds_read_b64_tr_b16 a[196:197], v176 offset:24576\n"
    "v_cvt_pk_bf16_f32 v156, v136, v137\n"
    "v_mfma_f32_16x16x32_bf16 v[116:119], a[68:71], a[32:35], v[116:119]\n"
    "ds_read_b64_tr_b16 a[198:199], v176 offset:27648\n"
    "v_cvt_pk_bf16_f32 v157, v138, v139\n"
    "v_mfma_f32_16x16x32_bf16 v[120:123], a[72:75], a[24:27], 0\n"
    "ds_read_b64_tr_b16 a[220:221], v179 offset:24576\n"
    "v_cvt_pk_bf16_f32 v158, v140, v141\n"
    "v_mfma_f32_16x16x32_bf16 v[120:123], a[76:79], a[28:31], v[120:123]\n"
    "ds_read_b64_tr_b16 a[222:223], v179 offset:27648\n"
    "v_cvt_pk_bf16_f32 v159, v142, v143\n"
    "v_mfma_f32_16x16x32_bf16 v[120:123], a[80:83], a[32:35], v[120:123]\n"
    "global_load_lds_dwordx4 v216, s[48:49] offset:0 sc1\n"
    "v_exp_f32 v112, v112\n"
    "v_mfma_f32_16x16x32_bf16 v[124:127], a[84:87], a[24:27], 0\n"
    "v_exp_f32 v113, v113\n"
    "v_mfma_f32_16x16x32_bf16 v[124:127], a[88:91], a[28:31], v[124:127]\n"
    "v_exp_f32 v114, v114\n"
    "v_mfma_f32_16x16x32_bf16 v[124:127], a[92:95], a[32:35], v[124:127]\n"
    "v_exp_f32 v115, v115\n"
    "v_smfmac_f32_16x16x64_bf16 v[52:55], a[240:243], v[152:159], v239\n"
    "ds_read_b64_tr_b16 a[200:201], v175 offset:24640\n"
    "ds_read_b64_tr_b16 a[202:203], v175 offset:27712\n"
    "v_mfma_f32_16x16x32_bf16 v[28:31], a[144:147], v[152:155], v[28:31]\n"
    "global_load_lds_dwordx4 v216, s[48:49] offset:1024 sc1\n"
    "v_exp_f32 v116, v116\n"
    "v_mfma_f32_16x16x32_bf16 v[28:31], a[168:171], v[156:159], v[28:31]\n"
    "v_exp_f32 v117, v117\n"
    "v_mfma_f32_16x16x32_bf16 v[32:35], a[148:151], v[152:155], v[32:35]\n"
    "v_exp_f32 v118, v118\n"
    "v_mfma_f32_16x16x32_bf16 v[32:35], a[172:175], v[156:159], v[32:35]\n"
    "ds_read_b64_tr_b16 a[224:225], v178 offset:24640\n"
    "v_exp_f32 v119, v119\n"
    "v_mfma_f32_16x16x32_bf16 v[36:39], a[152:155], v[152:155], v[36:39]\n"
    "global_load_lds_dwordx4 v216, s[48:49] offset:2048 sc1\n"
    "v_exp_f32 v120, v120\n"
    "v_mfma_f32_16x16x32_bf16 v[36:39], a[176:179], v[156:159], v[36:39]\n"
    "v_exp_f32 v121, v121\n"
    "v_mfma_f32_16x16x32_bf16 v[40:43], a[156:159], v[152:155], v[40:43]\n"
    "v_exp_f32 v122, v122\n"
    "v_mfma_f32_16x16x32_bf16 v[40:43], a[180:183], v[156:159], v[40:43]\n"
    "v_exp_f32 v123, v123\n"
    "v_mfma_f32_16x16x32_bf16 v[44:47], a[160:163], v[152:155], v[44:47]\n"
    "ds_read_b64_tr_b16 a[226:227], v178 offset:27712\n"
    "v_exp_f32 v124, v124\n"
    "v_mfma_f32_16x16x32_bf16 v[44:47], a[184:187], v[156:159], v[44:47]\n"
    "v_exp_f32 v125, v125\n"
    "v_mfma_f32_16x16x32_bf16 v[48:51], a[164:167], v[152:155], v[48:51]\n"
    "v_exp_f32 v126, v126\n"
    "v_mfma_f32_16x16x32_bf16 v[48:51], a[188:191], v[156:159], v[48:51]\n"
    "v_exp_f32 v127, v127\n"
    "v_mfma_f32_16x16x32_bf16 v[128:131], a[96:99], a[0:3], 0\n"
    "ds_read_b64_tr_b16 a[204:205], v176 offset:24640\n"
    "v_cvt_pk_bf16_f32 v152, v112, v113\n"
    "v_mfma_f32_16x16x32_bf16 v[128:131], a[100:103], a[4:7], v[128:131]\n"
    "ds_read_b64_tr_b16 a[206:207], v176 offset:27712\n"
    "v_cvt_pk_bf16_f32 v153, v114, v115\n"
    "v_mfma_f32_16x16x32_bf16 v[128:131], a[104:107], a[8:11], v[128:131]\n"
    "ds_read_b64_tr_b16 a[228:229], v179 offset:24640\n"
    "v_cvt_pk_bf16_f32 v154, v116, v117\n"
    "v_mfma_f32_16x16x32_bf16 v[132:135], a[108:111], a[0:3], 0\n"
    "ds_read_b64_tr_b16 a[230:231], v179 offset:27712\n"
    "v_cvt_pk_bf16_f32 v155, v118, v119\n"
    "v_mfma_f32_16x16x32_bf16 v[132:135], a[112:115], a[4:7], v[132:135]\n"
    "ds_read_b64_tr_b16 a[208:209], v175 offset:24704\n"
    "v_cvt_pk_bf16_f32 v156, v120, v121\n"
    "v_mfma_f32_16x16x32_bf16 v[132:135], a[116:119], a[8:11], v[132:135]\n"
    "ds_read_b64_tr_b16 a[210:211], v175 offset:27776\n"
    "v_cvt_pk_bf16_f32 v157, v122, v123\n"
    "v_mfma_f32_16x16x32_bf16 v[136:139], a[120:123], a[0:3], 0\n"
    "ds_read_b64_tr_b16 a[232:233], v178 offset:24704\n"
    "v_cvt_pk_bf16_f32 v158, v124, v125\n"
    "v_mfma_f32_16x16x32_bf16 v[136:139], a[124:127], a[4:7], v[136:139]\n"
    "s_waitcnt lgkmcnt(6)\n"
    "ds_read_b64_tr_b16 a[234:235], v178 offset:27776\n"
    "v_cvt_pk_bf16_f32 v159, v126, v127\n"
    "v_mfma_f32_16x16x32_bf16 v[136:139], a[128:131], a[8:11], v[136:139]\n"
    "global_load_dwordx2 v[224:225], v231, s[90:91] sc1\n"
    "v_exp_f32 v128, v128\n"
    "v_mfma_f32_16x16x32_bf16 v[140:143], a[132:135], a[0:3], 0\n"
    "v_exp_f32 v129, v129\n"
    "v_mfma_f32_16x16x32_bf16 v[140:143], a[136:139], a[4:7], v[140:143]\n"
    "v_exp_f32 v130, v130\n"
    "v_mfma_f32_16x16x32_bf16 v[140:143], a[140:143], a[8:11], v[140:143]\n"
    "v_exp_f32 v131, v131\n"
    "v_smfmac_f32_16x16x64_bf16 v[80:83], a[240:243], v[152:159], v239\n"
    "ds_read_b64_tr_b16 a[212:213], v176 offset:24704\n"
    "ds_read_b64_tr_b16 a[214:215], v176 offset:27776\n"
    "v_mfma_f32_16x16x32_bf16 v[56:59], a[144:147], v[152:155], v[56:59]\n"
    "v_exp_f32 v132, v132\n"
    "v_mfma_f32_16x16x32_bf16 v[56:59], a[168:171], v[156:159], v[56:59]\n"
    "v_exp_f32 v133, v133\n"
    "v_mfma_f32_16x16x32_bf16 v[60:63], a[148:151], v[152:155], v[60:63]\n"
    "v_exp_f32 v134, v134\n"
    "v_mfma_f32_16x16x32_bf16 v[60:63], a[172:175], v[156:159], v[60:63]\n"
    "ds_read_b64_tr_b16 a[236:237], v179 offset:24704\n"
    "v_exp_f32 v135, v135\n"
    "v_mfma_f32_16x16x32_bf16 v[64:67], a[152:155], v[152:155], v[64:67]\n"
    "v_exp_f32 v136, v136\n"
    "v_mfma_f32_16x16x32_bf16 v[64:67], a[176:179], v[156:159], v[64:67]\n"
    "v_exp_f32 v137, v137\n"
    "v_mfma_f32_16x16x32_bf16 v[68:71], a[156:159], v[152:155], v[68:71]\n"
    "v_exp_f32 v138, v138\n"
    "v_mfma_f32_16x16x32_bf16 v[68:71], a[180:183], v[156:159], v[68:71]\n"
    "v_exp_f32 v139, v139\n"
    "v_mfma_f32_16x16x32_bf16 v[72:75], a[160:163], v[152:155], v[72:75]\n"
    "ds_read_b64_tr_b16 a[238:239], v179 offset:27776\n"
    "v_exp_f32 v140, v140\n"
    "v_mfma_f32_16x16x32_bf16 v[72:75], a[184:187], v[156:159], v[72:75]\n"
    "v_exp_f32 v141, v141\n"
    "v_mfma_f32_16x16x32_bf16 v[76:79], a[164:167], v[152:155], v[76:79]\n"
    "v_exp_f32 v142, v142\n"
    "v_mfma_f32_16x16x32_bf16 v[76:79], a[188:191], v[156:159], v[76:79]\n"
    "v_exp_f32 v143, v143\n"
    "v_mfma_f32_16x16x32_bf16 v[112:115], a[96:99], a[12:15], 0\n"
    "s_waitcnt vmcnt(0) lgkmcnt(0)\n"
    "v_cvt_pk_bf16_f32 v152, v128, v129\n"
    "v_cvt_pk_bf16_f32 v153, v130, v131\n"
    "v_mfma_f32_16x16x32_bf16 v[112:115], a[100:103], a[16:19], v[112:115]\n"
    "s_barrier\n"
    "v_cvt_pk_bf16_f32 v154, v132, v133\n"
    "v_cvt_pk_bf16_f32 v155, v134, v135\n"
    "v_cmp_ne_u64 vcc, s[58:59], v[224:225]\n"
    "v_mfma_f32_16x16x32_bf16 v[112:115], a[104:107], a[20:23], v[112:115]\n"
    "ds_read_b128 a[48:51], v180 offset:0\n"
    "v_cvt_pk_bf16_f32 v156, v136, v137\n"
    "v_mfma_f32_16x16x32_bf16 v[116:119], a[108:111], a[12:15], 0\n"
    "ds_read_b128 a[52:55], v180 offset:64\n"
    "v_cvt_pk_bf16_f32 v157, v138, v139\n"
    "v_mfma_f32_16x16x32_bf16 v[116:119], a[112:115], a[16:19], v[116:119]\n"
    "ds_read_b128 a[56:59], v180 offset:128\n"
    "v_cvt_pk_bf16_f32 v158, v140, v141\n"
    "v_mfma_f32_16x16x32_bf16 v[116:119], a[116:119], a[20:23], v[116:119]\n"
    "ds_read_b128 a[60:63], v180 offset:3072\n"
    "v_cvt_pk_bf16_f32 v159, v142, v143\n"
    "v_mfma_f32_16x16x32_bf16 v[120:123], a[120:123], a[12:15], 0\n"
    "s_cmp_lg_u64 vcc, 0\n"
    "v_exp_f32 v112, v112\n"
    "v_mfma_f32_16x16x32_bf16 v[120:123], a[124:127], a[16:19], v[120:123]\n"
    "s_cbranch_scc1 Rd%=\n"
    "Bd%=:\n"
    "v_exp_f32 v113, v113\n"
    "v_mfma_f32_16x16x32_bf16 v[120:123], a[128:131], a[20:23], v[120:123]\n"
    "ds_read_b128 a[64:67], v180 offset:3136\n"
    "ds_read_b128 a[68:71], v180 offset:3200\n"
    "v_mfma_f32_16x16x32_bf16 v[124:127], a[132:135], a[12:15], 0\n"
    "v_exp_f32 v114, v114\n"
    "v_mfma_f32_16x16x32_bf16 v[124:127], a[136:139], a[16:19], v[124:127]\n"
    "s_mov_b64 exec, s[78:79]\n"
    "global_store_dwordx2 v227, v[240:241], s[84:85] sc1\n"
    "s_mov_b64 exec, -1\n"
    "v_exp_f32 v115, v115\n"
    "v_mfma_f32_16x16x32_bf16 v[124:127], a[140:143], a[20:23], v[124:127]\n"
    "v_exp_f32 v116, v116\n"
    "v_smfmac_f32_16x16x64_bf16 v[24:27], a[240:243], v[152:159], v239\n"
    "ds_read_b128 a[72:75], v183 offset:0\n"
    "ds_read_b128 a[76:79], v183 offset:64\n"
    "v_mfma_f32_16x16x32_bf16 v[0:3], a[192:195], v[152:155], v[0:3]\n"
    "v_exp_f32 v117, v117\n"
    "v_mfma_f32_16x16x32_bf16 v[0:3], a[216:219], v[156:159], v[0:3]\n"
    "v_exp_f32 v118, v118\n"
    "v_mfma_f32_16x16x32_bf16 v[4:7], a[196:199], v[152:155], v[4:7]\n"
    "v_exp_f32 v119, v119\n"
    "v_mfma_f32_16x16x32_bf16 v[4:7], a[220:223], v[156:159], v[4:7]\n"
    "v_exp_f32 v120, v120\n"
    "v_mfma_f32_16x16x32_bf16 v[8:11], a[200:203], v[152:155], v[8:11]\n"
    "ds_read_b128 a[80:83], v183 offset:128\n"
    "ds_read_b128 a[84:87], v183 offset:3072\n"
    "v_mfma_f32_16x16x32_bf16 v[8:11], a[224:227], v[156:159], v[8:11]\n"
    "v_exp_f32 v121, v121\n"
    "v_mfma_f32_16x16x32_bf16 v[12:15], a[204:207], v[152:155], v[12:15]\n"
    "v_exp_f32 v122, v122\n"
    "v_mfma_f32_16x16x32_bf16 v[12:15], a[228:231], v[156:159], v[12:15]\n"
    "ds_read_b128 a[88:91], v183 offset:3136\n"
    "v_exp_f32 v123, v123\n"
    "v_mfma_f32_16x16x32_bf16 v[16:19], a[208:211], v[152:155], v[16:19]\n"
    "v_exp_f32 v124, v124\n"
    "v_mfma_f32_16x16x32_bf16 v[16:19], a[232:235], v[156:159], v[16:19]\n"
    "v_exp_f32 v125, v125\n"
    "v_mfma_f32_16x16x32_bf16 v[20:23], a[212:215], v[152:155], v[20:23]\n"
    "ds_read_b128 a[92:95], v183 offset:3200\n"
    "v_exp_f32 v126, v126\n"
    "v_mfma_f32_16x16x32_bf16 v[20:23], a[236:239], v[156:159], v[20:23]\n"
    "v_exp_f32 v127, v127\n"
    "v_mfma_f32_16x16x32_bf16 v[128:131], a[96:99], a[24:27], 0\n"
    "ds_read_b64_tr_b16 a[144:145], v181 offset:0\n"
    "v_cvt_pk_bf16_f32 v152, v112, v113\n"
    "v_mfma_f32_16x16x32_bf16 v[128:131], a[100:103], a[28:31], v[128:131]\n"
    "ds_read_b64_tr_b16 a[146:147], v181 offset:3072\n"
    "v_cvt_pk_bf16_f32 v153, v114, v115\n"
    "v_mfma_f32_16x16x32_bf16 v[128:131], a[104:107], a[32:35], v[128:131]\n"
    "ds_read_b64_tr_b16 a[168:169], v184 offset:0\n"
    "v_cvt_pk_bf16_f32 v154, v116, v117\n"
    "v_mfma_f32_16x16x32_bf16 v[132:135], a[108:111], a[24:27], 0\n"
    "ds_read_b64_tr_b16 a[170:171], v184 offset:3072\n"
    "v_cvt_pk_bf16_f32 v155, v118, v119\n"
    "v_mfma_f32_16x16x32_bf16 v[132:135], a[112:115], a[28:31], v[132:135]\n"
    "ds_read_b64_tr_b16 a[148:149], v182 offset:0\n"
    "v_cvt_pk_bf16_f32 v156, v120, v121\n"
    "v_mfma_f32_16x16x32_bf16 v[132:135], a[116:119], a[32:35], v[132:135]\n"
    "ds_read_b64_tr_b16 a[150:151], v182 offset:3072\n"
    "v_cvt_pk_bf16_f32 v157, v122, v123\n"
    "v_mfma_f32_16x16x32_bf16 v[136:139], a[120:123], a[24:27], 0\n"
    "ds_read_b64_tr_b16 a[172:173], v185 offset:0\n"
    "v_cvt_pk_bf16_f32 v158, v124, v125\n"
    "v_mfma_f32_16x16x32_bf16 v[136:139], a[124:127], a[28:31], v[136:139]\n"
    "ds_read_b64_tr_b16 a[174:175], v185 offset:3072\n"
    "v_cvt_pk_bf16_f32 v159, v126, v127\n"
    "v_mfma_f32_16x16x32_bf16 v[136:139], a[128:131], a[32:35], v[136:139]\n"
    "v_exp_f32 v128, v128\n"
    "v_mfma_f32_16x16x32_bf16 v[140:143], a[132:135], a[24:27], 0\n"
    "v_exp_f32 v129, v129\n"
    "v_mfma_f32_16x16x32_bf16 v[140:143], a[136:139], a[28:31], v[140:143]\n"
    "v_exp_f32 v130, v130\n"
    "v_mfma_f32_16x16x32_bf16 v[140:143], a[140:143], a[32:35], v[140:143]\n"
    "v_exp_f32 v131, v131\n"
    "v_mfma_f32_16x16x32_bf16 v[144:147], a[120:123], a[36:39], v[164:167]\n"
    "v_exp_f32 v132, v132\n"
    "v_mfma_f32_16x16x32_bf16 v[144:147], a[124:127], a[40:43], v[144:147]\n"
    "v_exp_f32 v133, v133\n"
    "v_mfma_f32_16x16x32_bf16 v[144:147], a[128:131], a[44:47], v[144:147]\n"
    "v_exp_f32 v134, v134\n"
    "v_mfma_f32_16x16x32_bf16 v[148:151], a[132:135], a[36:39], v[164:167]\n"
    "v_exp_f32 v135, v135\n"
    "v_mfma_f32_16x16x32_bf16 v[148:151], a[136:139], a[40:43], v[148:151]\n"
    "v_exp_f32 v136, v136\n"
    "v_mfma_f32_16x16x32_bf16 v[148:151], a[140:143], a[44:47], v[148:151]\n"
    "v_exp_f32 v137, v137\n"
    "v_smfmac_f32_16x16x64_bf16 v[52:55], a[240:243], v[152:159], v239\n"
    "ds_read_b64_tr_b16 a[152:153], v181 offset:64\n"
    "ds_read_b64_tr_b16 a[154:155], v181 offset:3136\n"
    "v_mfma_f32_16x16x32_bf16 v[28:31], a[192:195], v[152:155], v[28:31]\n"
    "v_exp_f32 v138, v138\n"
    "v_mfma_f32_16x16x32_bf16 v[28:31], a[216:219], v[156:159], v[28:31]\n"
    "v_exp_f32 v139, v139\n"
    "v_mfma_f32_16x16x32_bf16 v[32:35], a[196:199], v[152:155], v[32:35]\n"
    "v_exp_f32 v140, v140\n"
    "v_mfma_f32_16x16x32_bf16 v[32:35], a[220:223], v[156:159], v[32:35]\n"
    "v_exp_f32 v141, v141\n"
    "v_mfma_f32_16x16x32_bf16 v[36:39], a[200:203], v[152:155], v[36:39]\n"
    "v_exp_f32 v142, v142\n"
    "v_mfma_f32_16x16x32_bf16 v[36:39], a[224:227], v[156:159], v[36:39]\n"
    "v_exp_f32 v143, v143\n"
    "v_mfma_f32_16x16x32_bf16 v[40:43], a[204:207], v[152:155], v[40:43]\n"
    "v_exp_f32 v144, v144\n"
    "v_mfma_f32_16x16x32_bf16 v[40:43], a[228:231], v[156:159], v[40:43]\n"
    "v_exp_f32 v145, v145\n"
    "v_mfma_f32_16x16x32_bf16 v[44:47], a[208:211], v[152:155], v[44:47]\n"
    "v_exp_f32 v146, v146\n"
    "v_mfma_f32_16x16x32_bf16 v[44:47], a[232:235], v[156:159], v[44:47]\n"
    "v_exp_f32 v147, v147\n"
    "v_mfma_f32_16x16x32_bf16 v[48:51], a[212:215], v[152:155], v[48:51]\n"
    "v_exp_f32 v148, v148\n"
    "v_mfma_f32_16x16x32_bf16 v[48:51], a[236:239], v[156:159], v[48:51]\n"
    "s_waitcnt lgkmcnt(0)\n"
    "v_exp_f32 v149, v149\n"
    "v_mfma_f32_16x16x32_bf16 v[112:115], a[48:51], a[0:3], 0\n"
    "ds_read_b64_tr_b16 a[176:177], v184 offset:64\n"
    "v_cvt_pk_bf16_f32 v152, v128, v129\n"
    "v_mfma_f32_16x16x32_bf16 v[112:115], a[52:55], a[4:7], v[112:115]\n"
    "ds_read_b64_tr_b16 a[178:179], v184 offset:3136\n"
    "v_cvt_pk_bf16_f32 v153, v130, v131\n"
    "v_mfma_f32_16x16x32_bf16 v[112:115], a[56:59], a[8:11], v[112:115]\n"
    "ds_read_b64_tr_b16 a[156:157], v182 offset:64\n"
    "v_cvt_pk_bf16_f32 v154, v132, v133\n"
    "v_mfma_f32_16x16x32_bf16 v[116:119], a[60:63], a[0:3], 0\n"
    "ds_read_b64_tr_b16 a[158:159], v182 offset:3136\n"
    "v_cvt_pk_bf16_f32 v155, v134, v135\n"
    "v_mfma_f32_16x16x32_bf16 v[116:119], a[64:67], a[4:7], v[116:119]\n"
    "ds_read_b64_tr_b16 a[180:181], v185 offset:64\n"
    "v_cvt_pk_bf16_f32 v156, v136, v137\n"
    "v_mfma_f32_16x16x32_bf16 v[116:119], a[68:71], a[8:11], v[116:119]\n"
    "ds_read_b64_tr_b16 a[182:183], v185 offset:3136\n"
    "v_cvt_pk_bf16_f32 v157, v138, v139\n"
    "v_mfma_f32_16x16x32_bf16 v[120:123], a[72:75], a[0:3], 0\n"
    "ds_read_b64_tr_b16 a[160:161], v181 offset:128\n"
    "v_cvt_pk_bf16_f32 v158, v140, v141\n"
    "v_mfma_f32_16x16x32_bf16 v[120:123], a[76:79], a[4:7], v[120:123]\n"
    "ds_read_b64_tr_b16 a[162:163], v181 offset:3200\n"
    "v_cvt_pk_bf16_f32 v159, v142, v143\n"
    "v_mfma_f32_16x16x32_bf16 v[120:123], a[80:83], a[8:11], v[120:123]\n"
    "v_exp_f32 v150, v150\n"
    "v_mfma_f32_16x16x32_bf16 v[124:127], a[84:87], a[0:3], 0\n"
    "v_exp_f32 v151, v151\n"
    "v_mfma_f32_16x16x32_bf16 v[124:127], a[88:91], a[4:7], v[124:127]\n"
    "ds_read_b64_tr_b16 a[184:185], v184 offset:128\n"
    "v_cvt_pk_bf16_f32 v160, v144, v145\n"
    "v_mfma_f32_16x16x32_bf16 v[124:127], a[92:95], a[8:11], v[124:127]\n"
    "ds_read_b64_tr_b16 a[186:187], v184 offset:3200\n"
    "v_cvt_pk_bf16_f32 v161, v146, v147\n"
    "v_smfmac_f32_16x16x64_bf16 v[80:83], a[240:243], v[152:159], v239\n"
    "ds_read_b64_tr_b16 a[164:165], v182 offset:128\n"
    "v_cvt_pk_bf16_f32 v162, v148, v149\n"
    "v_mfma_f32_16x16x32_bf16 v[56:59], a[192:195], v[152:155], v[56:59]\n"
    "ds_read_b64_tr_b16 a[166:167], v182 offset:3200\n"
    "v_cvt_pk_bf16_f32 v163, v150, v151\n"
    "v_mfma_f32_16x16x32_bf16 v[56:59], a[216:219], v[156:159], v[56:59]\n"
    "s_add_u32 m0, s40, 3072\n"
    "v_exp_f32 v112, v112\n"
    "v_mfma_f32_16x16x32_bf16 v[60:63], a[196:199], v[152:155], v[60:63]\n"
    "global_load_lds_dwordx4 v216, s[50:51] offset:-3072 sc1\n"
    "v_exp_f32 v113, v113\n"
    "v_mfma_f32_16x16x32_bf16 v[60:63], a[220:223], v[156:159], v[60:63]\n"
    "v_exp_f32 v114, v114\n"
    "v_mfma_f32_16x16x32_bf16 v[64:67], a[200:203], v[152:155], v[64:67]\n"
    "ds_read_b64_tr_b16 a[188:189], v185 offset:128\n"
    "global_load_lds_dwordx4 v216, s[50:51] offset:-2048 sc1\n"
    "v_mfma_f32_16x16x32_bf16 v[64:67], a[224:227], v[156:159], v[64:67]\n"
    "v_exp_f32 v115, v115\n"
    "v_mfma_f32_16x16x32_bf16 v[68:71], a[204:207], v[152:155], v[68:71]\n"
    "v_exp_f32 v116, v116\n"
    "v_mfma_f32_16x16x32_bf16 v[68:71], a[228:231], v[156:159], v[68:71]\n"
    "global_load_lds_dwordx4 v216, s[50:51] offset:-1024 sc1\n"
    "v_exp_f32 v117, v117\n"
    "v_mfma_f32_16x16x32_bf16 v[72:75], a[208:211], v[152:155], v[72:75]\n"
    "v_exp_f32 v118, v118\n"
    "v_mfma_f32_16x16x32_bf16 v[72:75], a[232:235], v[156:159], v[72:75]\n"
    "v_exp_f32 v119, v119\n"
    "v_mfma_f32_16x16x32_bf16 v[76:79], a[212:215], v[152:155], v[76:79]\n"
    "ds_read_b64_tr_b16 a[190:191], v185 offset:3200\n"
    "global_load_lds_dwordx4 v216, s[50:51] offset:0 sc1\n"
    "v_mfma_f32_16x16x32_bf16 v[76:79], a[236:239], v[156:159], v[76:79]\n"
    "v_exp_f32 v120, v120\n"
    "v_mfma_f32_16x16x32_bf16 v[84:87], a[216:219], v[160:163], v[84:87]\n"
    "v_exp_f32 v121, v121\n"
    "v_mfma_f32_16x16x32_bf16 v[88:91], a[220:223], v[160:163], v[88:91]\n"
    "global_load_lds_dwordx4 v216, s[50:51] offset:1024 sc1\n"
    "v_exp_f32 v122, v122\n"
    "v_mfma_f32_16x16x32_bf16 v[92:95], a[224:227], v[160:163], v[92:95]\n"
    "v_exp_f32 v123, v123\n"
    "v_mfma_f32_16x16x32_bf16 v[96:99], a[228:231], v[160:163], v[96:99]\n"
    "v_exp_f32 v124, v124\n"
    "v_mfma_f32_16x16x32_bf16 v[100:103], a[232:235], v[160:163], v[100:103]\n"
    "global_load_lds_dwordx4 v216, s[50:51] offset:2048 sc1\n"
    "v_exp_f32 v125, v125\n"
    "v_mfma_f32_16x16x32_bf16 v[104:107], a[236:239], v[160:163], v[104:107]\n"
    "v_exp_f32 v126, v126\n"
    "v_mfma_f32_16x16x32_bf16 v[108:111], a[240:243], v[160:163], v[108:111]\n"
    "v_exp_f32 v127, v127\n"
    "v_mfma_f32_16x16x32_bf16 v[128:131], a[48:51], a[12:15], 0\n"
    "ds_read_b128 a[96:99], v186 offset:0\n"
    "v_cvt_pk_bf16_f32 v152, v112, v113\n"
    "v_mfma_f32_16x16x32_bf16 v[128:131], a[52:55], a[16:19], v[128:131]\n"
    "ds_read_b128 a[100:103], v186 offset:64\n"
    "v_cvt_pk_bf16_f32 v153, v114, v115\n"
    "v_mfma_f32_16x16x32_bf16 v[128:131], a[56:59], a[20:23], v[128:131]\n"
    "ds_read_b128 a[104:107], v186 offset:128\n"
    "v_cvt_pk_bf16_f32 v154, v116, v117\n"
    "v_mfma_f32_16x16x32_bf16 v[132:135], a[60:63], a[12:15], 0\n"
    "ds_read_b128 a[108:111], v186 offset:3072\n"
    "v_cvt_pk_bf16_f32 v155, v118, v119\n"
    "v_mfma_f32_16x16x32_bf16 v[132:135], a[64:67], a[16:19], v[132:135]\n"
    "ds_read_b128 a[112:115], v186 offset:3136\n"
    "v_cvt_pk_bf16_f32 v156, v120, v121\n"
    "v_mfma_f32_16x16x32_bf16 v[132:135], a[68:71], a[20:23], v[132:135]\n"
    "ds_read_b128 a[116:119], v186 offset:3200\n"
    "v_cvt_pk_bf16_f32 v157, v122, v123\n"
    "v_mfma_f32_16x16x32_bf16 v[136:139], a[72:75], a[12:15], 0\n"
    "ds_read_b128 a[120:123], v189 offset:0\n"
    "v_cvt_pk_bf16_f32 v158, v124, v125\n"
    "v_mfma_f32_16x16x32_bf16 v[136:139], a[76:79], a[16:19], v[136:139]\n"
    "ds_read_b128 a[124:127], v189 offset:64\n"
    "v_cvt_pk_bf16_f32 v159, v126, v127\n"
    "v_mfma_f32_16x16x32_bf16 v[136:139], a[80:83], a[20:23], v[136:139]\n"
    "v_exp_f32 v128, v128\n"
    "v_mfma_f32_16x16x32_bf16 v[140:143], a[84:87], a[12:15], 0\n"
    "v_exp_f32 v129, v129\n"
    "v_mfma_f32_16x16x32_bf16 v[140:143], a[88:91], a[16:19], v[140:143]\n"
    "v_exp_f32 v130, v130\n"
    "v_mfma_f32_16x16x32_bf16 v[140:143], a[92:95], a[20:23], v[140:143]\n"
    "v_exp_f32 v131, v131\n"
    "v_smfmac_f32_16x16x64_bf16 v[24:27], a[240:243], v[152:159], v239\n"
    "ds_read_b128 a[128:131], v189 offset:128\n"
    "ds_read_b128 a[132:135], v189 offset:3072\n"
    "v_mfma_f32_16x16x32_bf16 v[0:3], a[144:147], v[152:155], v[0:3]\n"
    "v_exp_f32 v132, v132\n"
    "v_mfma_f32_16x16x32_bf16 v[0:3], a[168:171], v[156:159], v[0:3]\n"
    "v_exp_f32 v133, v133\n"
    "v_mfma_f32_16x16x32_bf16 v[4:7], a[148:151], v[152:155], v[4:7]\n"
    "v_exp_f32 v134, v134\n"
    "v_mfma_f32_16x16x32_bf16 v[4:7], a[172:175], v[156:159], v[4:7]\n"
    "ds_read_b128 a[136:139], v189 offset:3136\n"
    "v_exp_f32 v135, v135\n"
    "v_mfma_f32_16x16x32_bf16 v[8:11], a[152:155], v[152:155], v[8:11]\n"
    "s_waitcnt lgkmcnt(3)\n"
    "v_exp_f32 v136, v136\n"
    "v_mfma_f32_16x16x32_bf16 v[8:11], a[176:179], v[156:159], v[8:11]\n"
    "v_exp_f32 v137, v137\n"
    "v_mfma_f32_16x16x32_bf16 v[12:15], a[156:159], v[152:155], v[12:15]\n"
    "v_exp_f32 v138, v138\n"
    "v_mfma_f32_16x16x32_bf16 v[12:15], a[180:183], v[156:159], v[12:15]\n"
    "v_exp_f32 v139, v139\n"
    "v_mfma_f32_16x16x32_bf16 v[16:19], a[160:163], v[152:155], v[16:19]\n"
    "ds_read_b128 a[140:143], v189 offset:3200\n"
    "v_exp_f32 v140, v140\n"
    "v_mfma_f32_16x16x32_bf16 v[16:19], a[184:187], v[156:159], v[16:19]\n"
    "v_exp_f32 v141, v141\n"
    "v_mfma_f32_16x16x32_bf16 v[20:23], a[164:167], v[152:155], v[20:23]\n"
    "v_exp_f32 v142, v142\n"
    "v_mfma_f32_16x16x32_bf16 v[20:23], a[188:191], v[156:159], v[20:23]\n"
    "v_exp_f32 v143, v143\n"
    "v_mfma_f32_16x16x32_bf16 v[112:115], a[48:51], a[24:27], 0\n"
    "ds_read_b64_tr_b16 a[192:193], v187 offset:0\n"
    "v_cvt_pk_bf16_f32 v152, v128, v129\n"
    "v_mfma_f32_16x16x32_bf16 v[112:115], a[52:55], a[28:31], v[112:115]\n"
    "ds_read_b64_tr_b16 a[194:195], v187 offset:3072\n"
    "v_cvt_pk_bf16_f32 v153, v130, v131\n"
    "v_mfma_f32_16x16x32_bf16 v[112:115], a[56:59], a[32:35], v[112:115]\n"
    "ds_read_b64_tr_b16 a[216:217], v190 offset:0\n"
    "v_cvt_pk_bf16_f32 v154, v132, v133\n"
    "v_mfma_f32_16x16x32_bf16 v[116:119], a[60:63], a[24:27], 0\n"
    "ds_read_b64_tr_b16 a[218:219], v190 offset:3072\n"
    "v_cvt_pk_bf16_f32 v155, v134, v135\n"
    "v_mfma_f32_16x16x32_bf16 v[116:119], a[64:67], a[28:31], v[116:119]\n"
    "ds_read_b64_tr_b16 a[196:197], v188 offset:0\n"
    "v_cvt_pk_bf16_f32 v156, v136, v137\n"
    "v_mfma_f32_16x16x32_bf16 v[116:119], a[68:71], a[32:35], v[116:119]\n"
    "ds_read_b64_tr_b16 a[198:199], v188 offset:3072\n"
    "v_cvt_pk_bf16_f32 v157, v138, v139\n"
    "v_mfma_f32_16x16x32_bf16 v[120:123], a[72:75], a[24:27], 0\n"
    "ds_read_b64_tr_b16 a[220:221], v191 offset:0\n"
    "v_cvt_pk_bf16_f32 v158, v140, v141\n"
    "v_mfma_f32_16x16x32_bf16 v[120:123], a[76:79], a[28:31], v[120:123]\n"
    "ds_read_b64_tr_b16 a[222:223], v191 offset:3072\n"
    "v_cvt_pk_bf16_f32 v159, v142, v143\n"
    "v_mfma_f32_16x16x32_bf16 v[120:123], a[80:83], a[32:35], v[120:123]\n"
    "v_exp_f32 v112, v112\n"
    "v_mfma_f32_16x16x32_bf16 v[124:127], a[84:87], a[24:27], 0\n"
    "v_exp_f32 v113, v113\n"
    "v_mfma_f32_16x16x32_bf16 v[124:127], a[88:91], a[28:31], v[124:127]\n"
    "v_exp_f32 v114, v114\n"
    "v_mfma_f32_16x16x32_bf16 v[124:127], a[92:95], a[32:35], v[124:127]\n"
    "v_exp_f32 v115, v115\n"
    "v_smfmac_f32_16x16x64_bf16 v[52:55], a[240:243], v[152:159], v239\n"
    "ds_read_b64_tr_b16 a[200:201], v187 offset:64\n"
    "ds_read_b64_tr_b16 a[202:203], v187 offset:3136\n"
    "v_mfma_f32_16x16x32_bf16 v[28:31], a[144:147], v[152:155], v[28:31]\n"
    "v_exp_f32 v116, v116\n"
    "v_mfma_f32_16x16x32_bf16 v[28:31], a[168:171], v[156:159], v[28:31]\n"
    "v_exp_f32 v117, v117\n"
    "v_mfma_f32_16x16x32_bf16 v[32:35], a[148:151], v[152:155], v[32:35]\n"
    "v_exp_f32 v118, v118\n"
    "v_mfma_f32_16x16x32_bf16 v[32:35], a[172:175], v[156:159], v[32:35]\n"
    "ds_read_b64_tr_b16 a[224:225], v190 offset:64\n"
    "v_exp_f32 v119, v119\n"
    "v_mfma_f32_16x16x32_bf16 v[36:39], a[152:155], v[152:155], v[36:39]\n"
    "v_exp_f32 v120, v120\n"
    "v_mfma_f32_16x16x32_bf16 v[36:39], a[176:179], v[156:159], v[36:39]\n"
    "v_exp_f32 v121, v121\n"
    "v_mfma_f32_16x16x32_bf16 v[40:43], a[156:159], v[152:155], v[40:43]\n"
    "v_exp_f32 v122, v122\n"
    "v_mfma_f32_16x16x32_bf16 v[40:43], a[180:183], v[156:159], v[40:43]\n"
    "v_exp_f32 v123, v123\n"
    "v_mfma_f32_16x16x32_bf16 v[44:47], a[160:163], v[152:155], v[44:47]\n"
    "ds_read_b64_tr_b16 a[226:227], v190 offset:3136\n"
    "v_exp_f32 v124, v124\n"
    "v_mfma_f32_16x16x32_bf16 v[44:47], a[184:187], v[156:159], v[44:47]\n"
    "v_exp_f32 v125, v125\n"
    "v_mfma_f32_16x16x32_bf16 v[48:51], a[164:167], v[152:155], v[48:51]\n"
    "v_exp_f32 v126, v126\n"
    "v_mfma_f32_16x16x32_bf16 v[48:51], a[188:191], v[156:159], v[48:51]\n"
    "v_exp_f32 v127, v127\n"
    "v_mfma_f32_16x16x32_bf16 v[128:131], a[96:99], a[0:3], 0\n"
    "ds_read_b64_tr_b16 a[204:205], v188 offset:64\n"
    "v_cvt_pk_bf16_f32 v152, v112, v113\n"
    "v_mfma_f32_16x16x32_bf16 v[128:131], a[100:103], a[4:7], v[128:131]\n"
    "ds_read_b64_tr_b16 a[206:207], v188 offset:3136\n"
    "v_cvt_pk_bf16_f32 v153, v114, v115\n"
    "v_mfma_f32_16x16x32_bf16 v[128:131], a[104:107], a[8:11], v[128:131]\n"
    "ds_read_b64_tr_b16 a[228:229], v191 offset:64\n"
    "v_cvt_pk_bf16_f32 v154, v116, v117\n"
    "v_mfma_f32_16x16x32_bf16 v[132:135], a[108:111], a[0:3], 0\n"
    "ds_read_b64_tr_b16 a[230:231], v191 offset:3136\n"
    "v_cvt_pk_bf16_f32 v155, v118, v119\n"
    "v_mfma_f32_16x16x32_bf16 v[132:135], a[112:115], a[4:7], v[132:135]\n"
    "ds_read_b64_tr_b16 a[208:209], v187 offset:128\n"
    "v_cvt_pk_bf16_f32 v156, v120, v121\n"
    "v_mfma_f32_16x16x32_bf16 v[132:135], a[116:119], a[8:11], v[132:135]\n"
    "ds_read_b64_tr_b16 a[210:211], v187 offset:3200\n"
    "v_cvt_pk_bf16_f32 v157, v122, v123\n"
    "v_mfma_f32_16x16x32_bf16 v[136:139], a[120:123], a[0:3], 0\n"
    "ds_read_b64_tr_b16 a[232:233], v190 offset:128\n"
    "v_cvt_pk_bf16_f32 v158, v124, v125\n"
    "v_mfma_f32_16x16x32_bf16 v[136:139], a[124:127], a[4:7], v[136:139]\n"
    "s_waitcnt lgkmcnt(6)\n"
    "ds_read_b64_tr_b16 a[234:235], v190 offset:3200\n"
    "v_cvt_pk_bf16_f32 v159, v126, v127\n"
    "v_mfma_f32_16x16x32_bf16 v[136:139], a[128:131], a[8:11], v[136:139]\n"
    "v_exp_f32 v128, v128\n"
    "v_mfma_f32_16x16x32_bf16 v[140:143], a[132:135], a[0:3], 0\n"
    "v_exp_f32 v129, v129\n"
    "v_mfma_f32_16x16x32_bf16 v[140:143], a[136:139], a[4:7], v[140:143]\n"
    "v_exp_f32 v130, v130\n"
    "v_mfma_f32_16x16x32_bf16 v[140:143], a[140:143], a[8:11], v[140:143]\n"
    "v_exp_f32 v131, v131\n"
    "v_smfmac_f32_16x16x64_bf16 v[80:83], a[240:243], v[152:159], v239\n"
    "ds_read_b64_tr_b16 a[212:213], v188 offset:128\n"
    "ds_read_b64_tr_b16 a[214:215], v188 offset:3200\n"
    "v_mfma_f32_16x16x32_bf16 v[56:59], a[144:147], v[152:155], v[56:59]\n"
    "v_exp_f32 v132, v132\n"
    "v_mfma_f32_16x16x32_bf16 v[56:59], a[168:171], v[156:159], v[56:59]\n"
    "v_exp_f32 v133, v133\n"
    "v_mfma_f32_16x16x32_bf16 v[60:63], a[148:151], v[152:155], v[60:63]\n"
    "v_exp_f32 v134, v134\n"
    "v_mfma_f32_16x16x32_bf16 v[60:63], a[172:175], v[156:159], v[60:63]\n"
    "ds_read_b64_tr_b16 a[236:237], v191 offset:128\n"
    "v_exp_f32 v135, v135\n"
    "v_mfma_f32_16x16x32_bf16 v[64:67], a[152:155], v[152:155], v[64:67]\n"
    "v_exp_f32 v136, v136\n"
    "v_mfma_f32_16x16x32_bf16 v[64:67], a[176:179], v[156:159], v[64:67]\n"
    "v_exp_f32 v137, v137\n"
    "v_mfma_f32_16x16x32_bf16 v[68:71], a[156:159], v[152:155], v[68:71]\n"
    "v_exp_f32 v138, v138\n"
    "v_mfma_f32_16x16x32_bf16 v[68:71], a[180:183], v[156:159], v[68:71]\n"
    "v_exp_f32 v139, v139\n"
    "v_mfma_f32_16x16x32_bf16 v[72:75], a[160:163], v[152:155], v[72:75]\n"
    "ds_read_b64_tr_b16 a[238:239], v191 offset:3200\n"
    "v_exp_f32 v140, v140\n"
    "v_mfma_f32_16x16x32_bf16 v[72:75], a[184:187], v[156:159], v[72:75]\n"
    "v_exp_f32 v141, v141\n"
    "v_mfma_f32_16x16x32_bf16 v[76:79], a[164:167], v[152:155], v[76:79]\n"
    "v_exp_f32 v142, v142\n"
    "v_mfma_f32_16x16x32_bf16 v[76:79], a[188:191], v[156:159], v[76:79]\n"
    "v_exp_f32 v143, v143\n"
    "v_mfma_f32_16x16x32_bf16 v[112:115], a[96:99], a[12:15], 0\n"
    "ds_read_b128 a[48:51], v180 offset:24576\n"
    "v_cvt_pk_bf16_f32 v152, v128, v129\n"
    "v_mfma_f32_16x16x32_bf16 v[112:115], a[100:103], a[16:19], v[112:115]\n"
    "ds_read_b128 a[52:55], v180 offset:24640\n"
    "v_cvt_pk_bf16_f32 v153, v130, v131\n"
    "v_mfma_f32_16x16x32_bf16 v[112:115], a[104:107], a[20:23], v[112:115]\n"
    "ds_read_b128 a[56:59], v180 offset:24704\n"
    "v_cvt_pk_bf16_f32 v154, v132, v133\n"
    "v_mfma_f32_16x16x32_bf16 v[116:119], a[108:111], a[12:15], 0\n"
    "ds_read_b128 a[60:63], v180 offset:27648\n"
    "v_cvt_pk_bf16_f32 v155, v134, v135\n"
    "v_mfma_f32_16x16x32_bf16 v[116:119], a[112:115], a[16:19], v[116:119]\n"
    "ds_read_b128 a[64:67], v180 offset:27712\n"
    "v_cvt_pk_bf16_f32 v156, v136, v137\n"
    "v_mfma_f32_16x16x32_bf16 v[116:119], a[116:119], a[20:23], v[116:119]\n"
    "ds_read_b128 a[68:71], v180 offset:27776\n"
    "v_cvt_pk_bf16_f32 v157, v138, v139\n"
    "v_mfma_f32_16x16x32_bf16 v[120:123], a[120:123], a[12:15], 0\n"
    "ds_read_b128 a[72:75], v183 offset:24576\n"
    "v_cvt_pk_bf16_f32 v158, v140, v141\n"
    "v_mfma_f32_16x16x32_bf16 v[120:123], a[124:127], a[16:19], v[120:123]\n"
    "ds_read_b128 a[76:79], v183 offset:24640\n"
    "v_cvt_pk_bf16_f32 v159, v142, v143\n"
    "v_mfma_f32_16x16x32_bf16 v[120:123], a[128:131], a[20:23], v[120:123]\n"
    "v_exp_f32 v112, v112\n"
    "v_mfma_f32_16x16x32_bf16 v[124:127], a[132:135], a[12:15], 0\n"
    "v_exp_f32 v113, v113\n"
    "v_mfma_f32_16x16x32_bf16 v[124:127], a[136:139], a[16:19], v[124:127]\n"
    "v_exp_f32 v114, v114\n"
    "v_mfma_f32_16x16x32_bf16 v[124:127], a[140:143], a[20:23], v[124:127]\n"
    "v_exp_f32 v115, v115\n"
    "v_smfmac_f32_16x16x64_bf16 v[24:27], a[240:243], v[152:159], v239\n"
    "ds_read_b128 a[80:83], v183 offset:24704\n"
    "ds_read_b128 a[84:87], v183 offset:27648\n"
    "v_mfma_f32_16x16x32_bf16 v[0:3], a[192:195], v[152:155], v[0:3]\n"
    "v_exp_f32 v116, v116\n"
    "v_mfma_f32_16x16x32_bf16 v[0:3], a[216:219], v[156:159], v[0:3]\n"
    "v_exp_f32 v117, v117\n"
    "v_mfma_f32_16x16x32_bf16 v[4:7], a[196:199], v[152:155], v[4:7]\n"
    "v_exp_f32 v118, v118\n"
    "v_mfma_f32_16x16x32_bf16 v[4:7], a[220:223], v[156:159], v[4:7]\n"
    "ds_read_b128 a[88:91], v183 offset:27712\n"
    "v_exp_f32 v119, v119\n"
    "v_mfma_f32_16x16x32_bf16 v[8:11], a[200:203], v[152:155], v[8:11]\n"
    "v_exp_f32 v120, v120\n"
    "v_mfma_f32_16x16x32_bf16 v[8:11], a[224:227], v[156:159], v[8:11]\n"
    "s_waitcnt lgkmcnt(3)\n"
    "v_exp_f32 v121, v121\n"
    "v_mfma_f32_16x16x32_bf16 v[12:15], a[204:207], v[152:155], v[12:15]\n"
    "twins_p3:\n"
    "s_cbranch_execz twins_p7\n"
    "v_exp_f32 v122, v122\n"
    "v_mfma_f32_16x16x32_bf16 v[12:15], a[228:231], v[156:159], v[12:15]\n"
    "v_exp_f32 v123, v123\n"
    "v_mfma_f32_16x16x32_bf16 v[16:19], a[208:211], v[152:155], v[16:19]\n"
    "ds_read_b128 a[92:95], v183 offset:27776\n"
    "v_exp_f32 v124, v124\n"
    "v_mfma_f32_16x16x32_bf16 v[16:19], a[232:235], v[156:159], v[16:19]\n"
    "v_exp_f32 v125, v125\n"
    "v_mfma_f32_16x16x32_bf16 v[20:23], a[212:215], v[152:155], v[20:23]\n"
    "v_exp_f32 v126, v126\n"
    "v_mfma_f32_16x16x32_bf16 v[20:23], a[236:239], v[156:159], v[20:23]\n"
    "v_exp_f32 v127, v127\n"
    "v_mfma_f32_16x16x32_bf16 v[128:131], a[96:99], a[24:27], 0\n"
    "ds_read_b64_tr_b16 a[144:145], v181 offset:24576\n"
    "v_cvt_pk_bf16_f32 v152, v112, v113\n"
    "v_mfma_f32_16x16x32_bf16 v[128:131], a[100:103], a[28:31], v[128:131]\n"
    "ds_read_b64_tr_b16 a[146:147], v181 offset:27648\n"
    "v_cvt_pk_bf16_f32 v153, v114, v115\n"
    "v_mfma_f32_16x16x32_bf16 v[128:131], a[104:107], a[32:35], v[128:131]\n"
    "ds_read_b64_tr_b16 a[168:169], v184 offset:24576\n"
    "v_cvt_pk_bf16_f32 v154, v116, v117\n"
    "v_mfma_f32_16x16x32_bf16 v[132:135], a[108:111], a[24:27], 0\n"
    "ds_read_b64_tr_b16 a[170:171], v184 offset:27648\n"
    "v_cvt_pk_bf16_f32 v155, v118, v119\n"
    "v_mfma_f32_16x16x32_bf16 v[132:135], a[112:115], a[28:31], v[132:135]\n"
    "ds_read_b64_tr_b16 a[148:149], v182 offset:24576\n"
    "v_cvt_pk_bf16_f32 v156, v120, v121\n"
    "v_mfma_f32_16x16x32_bf16 v[132:135], a[116:119], a[32:35], v[132:135]\n"
    "ds_read_b64_tr_b16 a[150:151], v182 offset:27648\n"
    "v_cvt_pk_bf16_f32 v157, v122, v123\n"
    "v_mfma_f32_16x16x32_bf16 v[136:139], a[120:123], a[24:27], 0\n"
    "ds_read_b64_tr_b16 a[172:173], v185 offset:24576\n"
    "v_cvt_pk_bf16_f32 v158, v124, v125\n"
    "v_mfma_f32_16x16x32_bf16 v[136:139], a[124:127], a[28:31], v[136:139]\n"
    "ds_read_b64_tr_b16 a[174:175], v185 offset:27648\n"
    "v_cvt_pk_bf16_f32 v159, v126, v127\n"
    "v_mfma_f32_16x16x32_bf16 v[136:139], a[128:131], a[32:35], v[136:139]\n"
    "v_exp_f32 v128, v128\n"
    "v_mfma_f32_16x16x32_bf16 v[140:143], a[132:135], a[24:27], 0\n"
    "v_exp_f32 v129, v129\n"
    "v_mfma_f32_16x16x32_bf16 v[140:143], a[136:139], a[28:31], v[140:143]\n"
    "v_exp_f32 v130, v130\n"
    "v_mfma_f32_16x16x32_bf16 v[140:143], a[140:143], a[32:35], v[140:143]\n"
    "v_exp_f32 v131, v131\n"
    "v_mfma_f32_16x16x32_bf16 v[144:147], a[120:123], a[36:39], v[164:167]\n"
    "v_exp_f32 v132, v132\n"
    "v_mfma_f32_16x16x32_bf16 v[144:147], a[124:127], a[40:43], v[144:147]\n"
    "v_exp_f32 v133, v133\n"
    "v_mfma_f32_16x16x32_bf16 v[144:147], a[128:131], a[44:47], v[144:147]\n"
    "v_exp_f32 v134, v134\n"
    "v_mfma_f32_16x16x32_bf16 v[148:151], a[132:135], a[36:39], v[164:167]\n"
    "v_exp_f32 v135, v135\n"
    "v_mfma_f32_16x16x32_bf16 v[148:151], a[136:139], a[40:43], v[148:151]\n"
    "v_exp_f32 v136, v136\n"
    "v_mfma_f32_16x16x32_bf16 v[148:151], a[140:143], a[44:47], v[148:151]\n"
    "v_exp_f32 v137, v137\n"
    "v_smfmac_f32_16x16x64_bf16 v[52:55], a[240:243], v[152:159], v239\n"
    "ds_read_b64_tr_b16 a[152:153], v181 offset:24640\n"
    "ds_read_b64_tr_b16 a[154:155], v181 offset:27712\n"
    "v_mfma_f32_16x16x32_bf16 v[28:31], a[192:195], v[152:155], v[28:31]\n"
    "v_exp_f32 v138, v138\n"
    "v_mfma_f32_16x16x32_bf16 v[28:31], a[216:219], v[156:159], v[28:31]\n"
    "v_exp_f32 v139, v139\n"
    "v_mfma_f32_16x16x32_bf16 v[32:35], a[196:199], v[152:155], v[32:35]\n"
    "v_exp_f32 v140, v140\n"
    "v_mfma_f32_16x16x32_bf16 v[32:35], a[220:223], v[156:159], v[32:35]\n"
    "v_exp_f32 v141, v141\n"
    "v_mfma_f32_16x16x32_bf16 v[36:39], a[200:203], v[152:155], v[36:39]\n"
    "v_exp_f32 v142, v142\n"
    "v_mfma_f32_16x16x32_bf16 v[36:39], a[224:227], v[156:159], v[36:39]\n"
    "v_exp_f32 v143, v143\n"
    "v_mfma_f32_16x16x32_bf16 v[40:43], a[204:207], v[152:155], v[40:43]\n"
    "v_exp_f32 v144, v144\n"
    "v_mfma_f32_16x16x32_bf16 v[40:43], a[228:231], v[156:159], v[40:43]\n"
    "v_exp_f32 v145, v145\n"
    "v_mfma_f32_16x16x32_bf16 v[44:47], a[208:211], v[152:155], v[44:47]\n"
    "v_exp_f32 v146, v146\n"
    "v_mfma_f32_16x16x32_bf16 v[44:47], a[232:235], v[156:159], v[44:47]\n"
    "v_exp_f32 v147, v147\n"
    "v_mfma_f32_16x16x32_bf16 v[48:51], a[212:215], v[152:155], v[48:51]\n"
    "v_exp_f32 v148, v148\n"
    "v_mfma_f32_16x16x32_bf16 v[48:51], a[236:239], v[156:159], v[48:51]\n"
    "v_exp_f32 v149, v149\n"
    "v_mfma_f32_16x16x32_bf16 v[112:115], a[48:51], a[0:3], 0\n"
    "ds_read_b64_tr_b16 a[176:177], v184 offset:24640\n"
    "v_cvt_pk_bf16_f32 v152, v128, v129\n"
    "v_mfma_f32_16x16x32_bf16 v[112:115], a[52:55], a[4:7], v[112:115]\n"
    "ds_read_b64_tr_b16 a[178:179], v184 offset:27712\n"
    "v_cvt_pk_bf16_f32 v153, v130, v131\n"
    "v_mfma_f32_16x16x32_bf16 v[112:115], a[56:59], a[8:11], v[112:115]\n"
    "ds_read_b64_tr_b16 a[156:157], v182 offset:24640\n"
    "v_cvt_pk_bf16_f32 v154, v132, v133\n"
    "v_mfma_f32_16x16x32_bf16 v[116:119], a[60:63], a[0:3], 0\n"
    "ds_read_b64_tr_b16 a[158:159], v182 offset:27712\n"
    "v_cvt_pk_bf16_f32 v155, v134, v135\n"
    "v_mfma_f32_16x16x32_bf16 v[116:119], a[64:67], a[4:7], v[116:119]\n"
    "ds_read_b64_tr_b16 a[180:181], v185 offset:24640\n"
    "v_cvt_pk_bf16_f32 v156, v136, v137\n"
    "v_mfma_f32_16x16x32_bf16 v[116:119], a[68:71], a[8:11], v[116:119]\n"
    "ds_read_b64_tr_b16 a[182:183], v185 offset:27712\n"
    "v_cvt_pk_bf16_f32 v157, v138, v139\n"
    "v_mfma_f32_16x16x32_bf16 v[120:123], a[72:75], a[0:3], 0\n"
    "ds_read_b64_tr_b16 a[160:161], v181 offset:24704\n"
    "v_cvt_pk_bf16_f32 v158, v140, v141\n"
    "v_mfma_f32_16x16x32_bf16 v[120:123], a[76:79], a[4:7], v[120:123]\n"
    "s_waitcnt lgkmcnt(6)\n"
    "ds_read_b64_tr_b16 a[162:163], v181 offset:27776\n"
    "v_cvt_pk_bf16_f32 v159, v142, v143\n"
    "v_mfma_f32_16x16x32_bf16 v[120:123], a[80:83], a[8:11], v[120:123]\n"
    "v_exp_f32 v150, v150\n"
    "v_mfma_f32_16x16x32_bf16 v[124:127], a[84:87], a[0:3], 0\n"
    "v_exp_f32 v151, v151\n"
    "v_mfma_f32_16x16x32_bf16 v[124:127], a[88:91], a[4:7], v[124:127]\n"
    "ds_read_b64_tr_b16 a[184:185], v184 offset:24704\n"
    "v_cvt_pk_bf16_f32 v160, v144, v145\n"
    "v_mfma_f32_16x16x32_bf16 v[124:127], a[92:95], a[8:11], v[124:127]\n"
    "ds_read_b64_tr_b16 a[186:187], v184 offset:27776\n"
    "v_cvt_pk_bf16_f32 v161, v146, v147\n"
    "v_smfmac_f32_16x16x64_bf16 v[80:83], a[240:243], v[152:159], v239\n"
    "ds_read_b64_tr_b16 a[164:165], v182 offset:24704\n"
    "v_cvt_pk_bf16_f32 v162, v148, v149\n"
    "v_mfma_f32_16x16x32_bf16 v[56:59], a[192:195], v[152:155], v[56:59]\n"
    "ds_read_b64_tr_b16 a[166:167], v182 offset:27776\n"
    "v_cvt_pk_bf16_f32 v163, v150, v151\n"
    "v_mfma_f32_16x16x32_bf16 v[56:59], a[216:219], v[156:159], v[56:59]\n"
    "s_add_u32 m0, s40, 27648\n"
    "v_exp_f32 v112, v112\n"
    "v_mfma_f32_16x16x32_bf16 v[60:63], a[196:199], v[152:155], v[60:63]\n"
    "global_load_lds_dwordx4 v216, s[52:53] offset:-3072 sc1\n"
    "v_exp_f32 v113, v113\n"
    "v_mfma_f32_16x16x32_bf16 v[60:63], a[220:223], v[156:159], v[60:63]\n"
    "v_exp_f32 v114, v114\n"
    "v_mfma_f32_16x16x32_bf16 v[64:67], a[200:203], v[152:155], v[64:67]\n"
    "ds_read_b64_tr_b16 a[188:189], v185 offset:24704\n"
    "global_load_lds_dwordx4 v216, s[52:53] offset:-2048 sc1\n"
    "v_mfma_f32_16x16x32_bf16 v[64:67], a[224:227], v[156:159], v[64:67]\n"
    "v_exp_f32 v115, v115\n"
    "v_mfma_f32_16x16x32_bf16 v[68:71], a[204:207], v[152:155], v[68:71]\n"
    "v_exp_f32 v116, v116\n"
    "v_mfma_f32_16x16x32_bf16 v[68:71], a[228:231], v[156:159], v[68:71]\n"
    "global_load_lds_dwordx4 v216, s[52:53] offset:-1024 sc1\n"
    "v_exp_f32 v117, v117\n"
    "v_mfma_f32_16x16x32_bf16 v[72:75], a[208:211], v[152:155], v[72:75]\n"
    "v_exp_f32 v118, v118\n"
    "v_mfma_f32_16x16x32_bf16 v[72:75], a[232:235], v[156:159], v[72:75]\n"
    "v_exp_f32 v119, v119\n"
    "v_mfma_f32_16x16x32_bf16 v[76:79], a[212:215], v[152:155], v[76:79]\n"
    "ds_read_b64_tr_b16 a[190:191], v185 offset:27776\n"
    "global_load_lds_dwordx4 v216, s[52:53] offset:0 sc1\n"
    "v_mfma_f32_16x16x32_bf16 v[76:79], a[236:239], v[156:159], v[76:79]\n"
    "v_exp_f32 v120, v120\n"
    "v_mfma_f32_16x16x32_bf16 v[84:87], a[216:219], v[160:163], v[84:87]\n"
    "v_exp_f32 v121, v121\n"
    "v_mfma_f32_16x16x32_bf16 v[88:91], a[220:223], v[160:163], v[88:91]\n"
    "global_load_lds_dwordx4 v216, s[52:53] offset:1024 sc1\n"
    "v_exp_f32 v122, v122\n"
    "v_mfma_f32_16x16x32_bf16 v[92:95], a[224:227], v[160:163], v[92:95]\n"
    "v_exp_f32 v123, v123\n"
    "v_mfma_f32_16x16x32_bf16 v[96:99], a[228:231], v[160:163], v[96:99]\n"
    "v_exp_f32 v124, v124\n"
    "v_mfma_f32_16x16x32_bf16 v[100:103], a[232:235], v[160:163], v[100:103]\n"
    "global_load_lds_dwordx4 v216, s[52:53] offset:2048 sc1\n"
    "v_exp_f32 v125, v125\n"
    "v_mfma_f32_16x16x32_bf16 v[104:107], a[236:239], v[160:163], v[104:107]\n"
    "v_exp_f32 v126, v126\n"
    "v_mfma_f32_16x16x32_bf16 v[108:111], a[240:243], v[160:163], v[108:111]\n"
    "v_exp_f32 v127, v127\n"
    "v_mfma_f32_16x16x32_bf16 v[128:131], a[48:51], a[12:15], 0\n"
    "ds_read_b128 a[96:99], v186 offset:24576\n"
    "v_cvt_pk_bf16_f32 v152, v112, v113\n"
    "v_mfma_f32_16x16x32_bf16 v[128:131], a[52:55], a[16:19], v[128:131]\n"
    "ds_read_b128 a[100:103], v186 offset:24640\n"
    "v_cvt_pk_bf16_f32 v153, v114, v115\n"
    "v_mfma_f32_16x16x32_bf16 v[128:131], a[56:59], a[20:23], v[128:131]\n"
    "ds_read_b128 a[104:107], v186 offset:24704\n"
    "v_cvt_pk_bf16_f32 v154, v116, v117\n"
    "v_mfma_f32_16x16x32_bf16 v[132:135], a[60:63], a[12:15], 0\n"
    "ds_read_b128 a[108:111], v186 offset:27648\n"
    "v_cvt_pk_bf16_f32 v155, v118, v119\n"
    "v_mfma_f32_16x16x32_bf16 v[132:135], a[64:67], a[16:19], v[132:135]\n"
    "ds_read_b128 a[112:115], v186 offset:27712\n"
    "v_cvt_pk_bf16_f32 v156, v120, v121\n"
    "v_mfma_f32_16x16x32_bf16 v[132:135], a[68:71], a[20:23], v[132:135]\n"
    "ds_read_b128 a[116:119], v186 offset:27776\n"
    "v_cvt_pk_bf16_f32 v157, v122, v123\n"
    "v_mfma_f32_16x16x32_bf16 v[136:139], a[72:75], a[12:15], 0\n"
    "ds_read_b128 a[120:123], v189 offset:24576\n"
    "v_cvt_pk_bf16_f32 v158, v124, v125\n"
    "v_mfma_f32_16x16x32_bf16 v[136:139], a[76:79], a[16:19], v[136:139]\n"
    "ds_read_b128 a[124:127], v189 offset:24640\n"
    "v_cvt_pk_bf16_f32 v159, v126, v127\n"
    "v_mfma_f32_16x16x32_bf16 v[136:139], a[80:83], a[20:23], v[136:139]\n"
    "v_exp_f32 v128, v128\n"
    "v_mfma_f32_16x16x32_bf16 v[140:143], a[84:87], a[12:15], 0\n"
    "global_load_dwordx2 v[224:225], v231, s[92:93] sc1\n"
    "v_exp_f32 v129, v129\n"
    "v_mfma_f32_16x16x32_bf16 v[140:143], a[88:91], a[16:19], v[140:143]\n"
    "v_exp_f32 v130, v130\n"
    "v_mfma_f32_16x16x32_bf16 v[140:143], a[92:95], a[20:23], v[140:143]\n"
    "v_exp_f32 v131, v131\n"
    "v_smfmac_f32_16x16x64_bf16 v[24:27], a[240:243], v[152:159], v239\n"
    "ds_read_b128 a[128:131], v189 offset:24704\n"
    "ds_read_b128 a[132:135], v189 offset:27648\n"
    "v_mfma_f32_16x16x32_bf16 v[0:3], a[144:147], v[152:155], v[0:3]\n"
    "v_exp_f32 v132, v132\n"
    "v_mfma_f32_16x16x32_bf16 v[0:3], a[168:171], v[156:159], v[0:3]\n"
    "v_exp_f32 v133, v133\n"
    "v_mfma_f32_16x16x32_bf16 v[4:7], a[148:151], v[152:155], v[4:7]\n"
    "v_exp_f32 v134, v134\n"
    "v_mfma_f32_16x16x32_bf16 v[4:7], a[172:175], v[156:159], v[4:7]\n"
    "ds_read_b128 a[136:139], v189 offset:27712\n"
    "v_exp_f32 v135, v135\n"
    "v_mfma_f32_16x16x32_bf16 v[8:11], a[152:155], v[152:155], v[8:11]\n"
    "s_waitcnt lgkmcnt(3)\n"
    "v_exp_f32 v136, v136\n"
    "v_mfma_f32_16x16x32_bf16 v[8:11], a[176:179], v[156:159], v[8:11]\n"
    "v_exp_f32 v137, v137\n"
    "v_mfma_f32_16x16x32_bf16 v[12:15], a[156:159], v[152:155], v[12:15]\n"
    "v_exp_f32 v138, v138\n"
    "v_mfma_f32_16x16x32_bf16 v[12:15], a[180:183], v[156:159], v[12:15]\n"
    "v_exp_f32 v139, v139\n"
    "v_mfma_f32_16x16x32_bf16 v[16:19], a[160:163], v[152:155], v[16:19]\n"
    "ds_read_b128 a[140:143], v189 offset:27776\n"
    "v_exp_f32 v140, v140\n"
    "v_mfma_f32_16x16x32_bf16 v[16:19], a[184:187], v[156:159], v[16:19]\n"
    "v_exp_f32 v141, v141\n"
    "v_mfma_f32_16x16x32_bf16 v[20:23], a[164:167], v[152:155], v[20:23]\n"
    "v_exp_f32 v142, v142\n"
    "v_mfma_f32_16x16x32_bf16 v[20:23], a[188:191], v[156:159], v[20:23]\n"
    "v_exp_f32 v143, v143\n"
    "v_mfma_f32_16x16x32_bf16 v[112:115], a[48:51], a[24:27], 0\n"
    "ds_read_b64_tr_b16 a[192:193], v187 offset:24576\n"
    "v_cvt_pk_bf16_f32 v152, v128, v129\n"
    "v_mfma_f32_16x16x32_bf16 v[112:115], a[52:55], a[28:31], v[112:115]\n"
    "ds_read_b64_tr_b16 a[194:195], v187 offset:27648\n"
    "v_cvt_pk_bf16_f32 v153, v130, v131\n"
    "v_mfma_f32_16x16x32_bf16 v[112:115], a[56:59], a[32:35], v[112:115]\n"
    "ds_read_b64_tr_b16 a[216:217], v190 offset:24576\n"
    "v_cvt_pk_bf16_f32 v154, v132, v133\n"
    "v_mfma_f32_16x16x32_bf16 v[116:119], a[60:63], a[24:27], 0\n"
    "ds_read_b64_tr_b16 a[218:219], v190 offset:27648\n"
    "v_cvt_pk_bf16_f32 v155, v134, v135\n"
    "v_mfma_f32_16x16x32_bf16 v[116:119], a[64:67], a[28:31], v[116:119]\n"
    "ds_read_b64_tr_b16 a[196:197], v188 offset:24576\n"
    "v_cvt_pk_bf16_f32 v156, v136, v137\n"
    "v_mfma_f32_16x16x32_bf16 v[116:119], a[68:71], a[32:35], v[116:119]\n"
    "ds_read_b64_tr_b16 a[198:199], v188 offset:27648\n"
    "v_cvt_pk_bf16_f32 v157, v138, v139\n"
    "v_mfma_f32_16x16x32_bf16 v[120:123], a[72:75], a[24:27], 0\n"
    "ds_read_b64_tr_b16 a[220:221], v191 offset:24576\n"
    "v_cvt_pk_bf16_f32 v158, v140, v141\n"
    "v_mfma_f32_16x16x32_bf16 v[120:123], a[76:79], a[28:31], v[120:123]\n"
    "ds_read_b64_tr_b16 a[222:223], v191 offset:27648\n"
    "v_cvt_pk_bf16_f32 v159, v142, v143\n"
    "v_mfma_f32_16x16x32_bf16 v[120:123], a[80:83], a[32:35], v[120:123]\n"
    "v_exp_f32 v112, v112\n"
    "v_mfma_f32_16x16x32_bf16 v[124:127], a[84:87], a[24:27], 0\n"
    "v_exp_f32 v113, v113\n"
    "v_mfma_f32_16x16x32_bf16 v[124:127], a[88:91], a[28:31], v[124:127]\n"
    "v_exp_f32 v114, v114\n"
    "v_mfma_f32_16x16x32_bf16 v[124:127], a[92:95], a[32:35], v[124:127]\n"
    "v_exp_f32 v115, v115\n"
    "v_smfmac_f32_16x16x64_bf16 v[52:55], a[240:243], v[152:159], v239\n"
    "ds_read_b64_tr_b16 a[200:201], v187 offset:24640\n"
    "ds_read_b64_tr_b16 a[202:203], v187 offset:27712\n"
    "v_mfma_f32_16x16x32_bf16 v[28:31], a[144:147], v[152:155], v[28:31]\n"
    "v_exp_f32 v116, v116\n"
    "v_mfma_f32_16x16x32_bf16 v[28:31], a[168:171], v[156:159], v[28:31]\n"
    "v_exp_f32 v117, v117\n"
    "v_mfma_f32_16x16x32_bf16 v[32:35], a[148:151], v[152:155], v[32:35]\n"
    "v_exp_f32 v118, v118\n"
    "v_mfma_f32_16x16x32_bf16 v[32:35], a[172:175], v[156:159], v[32:35]\n"
    "ds_read_b64_tr_b16 a[224:225], v190 offset:24640\n"
    "v_exp_f32 v119, v119\n"
    "v_mfma_f32_16x16x32_bf16 v[36:39], a[152:155], v[152:155], v[36:39]\n"
    "v_exp_f32 v120, v120\n"
    "v_mfma_f32_16x16x32_bf16 v[36:39], a[176:179], v[156:159], v[36:39]\n"
    "v_exp_f32 v121, v121\n"
    "v_mfma_f32_16x16x32_bf16 v[40:43], a[156:159], v[152:155], v[40:43]\n"
    "v_exp_f32 v122, v122\n"
    "v_mfma_f32_16x16x32_bf16 v[40:43], a[180:183], v[156:159], v[40:43]\n"
    "v_exp_f32 v123, v123\n"
    "v_mfma_f32_16x16x32_bf16 v[44:47], a[160:163], v[152:155], v[44:47]\n"
    "ds_read_b64_tr_b16 a[226:227], v190 offset:27712\n"
    "v_exp_f32 v124, v124\n"
    "v_mfma_f32_16x16x32_bf16 v[44:47], a[184:187], v[156:159], v[44:47]\n"
    "v_exp_f32 v125, v125\n"
    "v_mfma_f32_16x16x32_bf16 v[48:51], a[164:167], v[152:155], v[48:51]\n"
    "v_exp_f32 v126, v126\n"
    "v_mfma_f32_16x16x32_bf16 v[48:51], a[188:191], v[156:159], v[48:51]\n"
    "v_exp_f32 v127, v127\n"
    "v_mfma_f32_16x16x32_bf16 v[128:131], a[96:99], a[0:3], 0\n"
    "ds_read_b64_tr_b16 a[204:205], v188 offset:24640\n"
    "v_cvt_pk_bf16_f32 v152, v112, v113\n"
    "v_mfma_f32_16x16x32_bf16 v[128:131], a[100:103], a[4:7], v[128:131]\n"
    "ds_read_b64_tr_b16 a[206:207], v188 offset:27712\n"
    "v_cvt_pk_bf16_f32 v153, v114, v115\n"
    "v_mfma_f32_16x16x32_bf16 v[128:131], a[104:107], a[8:11], v[128:131]\n"
    "ds_read_b64_tr_b16 a[228:229], v191 offset:24640\n"
    "v_cvt_pk_bf16_f32 v154, v116, v117\n"
    "v_mfma_f32_16x16x32_bf16 v[132:135], a[108:111], a[0:3], 0\n"
    "ds_read_b64_tr_b16 a[230:231], v191 offset:27712\n"
    "v_cvt_pk_bf16_f32 v155, v118, v119\n"
    "v_mfma_f32_16x16x32_bf16 v[132:135], a[112:115], a[4:7], v[132:135]\n"
    "ds_read_b64_tr_b16 a[208:209], v187 offset:24704\n"
    "v_cvt_pk_bf16_f32 v156, v120, v121\n"
    "v_mfma_f32_16x16x32_bf16 v[132:135], a[116:119], a[8:11], v[132:135]\n"
    "ds_read_b64_tr_b16 a[210:211], v187 offset:27776\n"
    "v_cvt_pk_bf16_f32 v157, v122, v123\n"
    "v_mfma_f32_16x16x32_bf16 v[136:139], a[120:123], a[0:3], 0\n"
    "ds_read_b64_tr_b16 a[232:233], v190 offset:24704\n"
    "v_cvt_pk_bf16_f32 v158, v124, v125\n"
    "v_mfma_f32_16x16x32_bf16 v[136:139], a[124:127], a[4:7], v[136:139]\n"
    "s_waitcnt lgkmcnt(6)\n"
    "ds_read_b64_tr_b16 a[234:235], v190 offset:27776\n"
    "v_cvt_pk_bf16_f32 v159, v126, v127\n"
    "v_mfma_f32_16x16x32_bf16 v[136:139], a[128:131], a[8:11], v[136:139]\n"
    "v_exp_f32 v128, v128\n"
    "v_mfma_f32_16x16x32_bf16 v[140:143], a[132:135], a[0:3], 0\n"
    "v_exp_f32 v129, v129\n"
    "v_mfma_f32_16x16x32_bf16 v[140:143], a[136:139], a[4:7], v[140:143]\n"
    "v_exp_f32 v130, v130\n"
    "v_mfma_f32_16x16x32_bf16 v[140:143], a[140:143], a[8:11], v[140:143]\n"
    "v_exp_f32 v131, v131\n"
    "v_smfmac_f32_16x16x64_bf16 v[80:83], a[240:243], v[152:159], v239\n"
    "ds_read_b64_tr_b16 a[212:213], v188 offset:24704\n"
    "ds_read_b64_tr_b16 a[214:215], v188 offset:27776\n"
    "v_mfma_f32_16x16x32_bf16 v[56:59], a[144:147], v[152:155], v[56:59]\n"
    "v_exp_f32 v132, v132\n"
    "v_mfma_f32_16x16x32_bf16 v[56:59], a[168:171], v[156:159], v[56:59]\n"
    "v_exp_f32 v133, v133\n"
    "v_mfma_f32_16x16x32_bf16 v[60:63], a[148:151], v[152:155], v[60:63]\n"
    "v_exp_f32 v134, v134\n"
    "v_mfma_f32_16x16x32_bf16 v[60:63], a[172:175], v[156:159], v[60:63]\n"
    "ds_read_b64_tr_b16 a[236:237], v191 offset:24704\n"
    "v_exp_f32 v135, v135\n"
    "v_mfma_f32_16x16x32_bf16 v[64:67], a[152:155], v[152:155], v[64:67]\n"
    "v_exp_f32 v136, v136\n"
    "v_mfma_f32_16x16x32_bf16 v[64:67], a[176:179], v[156:159], v[64:67]\n"
    "v_exp_f32 v137, v137\n"
    "v_mfma_f32_16x16x32_bf16 v[68:71], a[156:159], v[152:155], v[68:71]\n"
    "v_exp_f32 v138, v138\n"
    "v_mfma_f32_16x16x32_bf16 v[68:71], a[180:183], v[156:159], v[68:71]\n"
    "v_exp_f32 v139, v139\n"
    "v_mfma_f32_16x16x32_bf16 v[72:75], a[160:163], v[152:155], v[72:75]\n"
    "ds_read_b64_tr_b16 a[238:239], v191 offset:27776\n"
    "v_exp_f32 v140, v140\n"
    "v_mfma_f32_16x16x32_bf16 v[72:75], a[184:187], v[156:159], v[72:75]\n"
    "v_exp_f32 v141, v141\n"
    "v_mfma_f32_16x16x32_bf16 v[76:79], a[164:167], v[152:155], v[76:79]\n"
    "v_exp_f32 v142, v142\n"
    "v_mfma_f32_16x16x32_bf16 v[76:79], a[188:191], v[156:159], v[76:79]\n"
    "v_exp_f32 v143, v143\n"
    "v_mfma_f32_16x16x32_bf16 v[112:115], a[96:99], a[12:15], 0\n"
    "s_waitcnt vmcnt(0) lgkmcnt(0)\n"
    "v_cvt_pk_bf16_f32 v152, v128, v129\n"
    "v_cvt_pk_bf16_f32 v153, v130, v131\n"
    "v_mfma_f32_16x16x32_bf16 v[112:115], a[100:103], a[16:19], v[112:115]\n"
    "s_barrier\n"
    "v_cvt_pk_bf16_f32 v154, v132, v133\n"
    "v_cvt_pk_bf16_f32 v155, v134, v135\n"
    "v_cmp_ne_u64 vcc, s[58:59], v[224:225]\n"
    "v_mfma_f32_16x16x32_bf16 v[112:115], a[104:107], a[20:23], v[112:115]\n"
    "ds_read_b128 a[48:51], v168 offset:0\n"
    "v_cvt_pk_bf16_f32 v156, v136, v137\n"
    "v_mfma_f32_16x16x32_bf16 v[116:119], a[108:111], a[12:15], 0\n"
    "ds_read_b128 a[52:55], v168 offset:64\n"
    "v_cvt_pk_bf16_f32 v157, v138, v139\n"
    "v_mfma_f32_16x16x32_bf16 v[116:119], a[112:115], a[16:19], v[116:119]\n"
    "ds_read_b128 a[56:59], v168 offset:128\n"
    "v_cvt_pk_bf16_f32 v158, v140, v141\n"
    "v_mfma_f32_16x16x32_bf16 v[116:119], a[116:119], a[20:23], v[116:119]\n"
    "ds_read_b128 a[60:63], v168 offset:3072\n"
    "v_cvt_pk_bf16_f32 v159, v142, v143\n"
    "v_mfma_f32_16x16x32_bf16 v[120:123], a[120:123], a[12:15], 0\n"
    "s_and_b32 s38, vcc_hi, 0x3ffff\n"
    "v_exp_f32 v112, v112\n"
    "v_mfma_f32_16x16x32_bf16 v[120:123], a[124:127], a[16:19], v[120:123]\n"
    "s_cbranch_scc1 Rv%=\n"
    "Bv%=:\n"
    "v_exp_f32 v113, v113\n"
    "v_mfma_f32_16x16x32_bf16 v[120:123], a[128:131], a[20:23], v[120:123]\n"
    "ds_read_b128 a[64:67], v168 offset:3136\n"
    "ds_read_b128 a[68:71], v168 offset:3200\n"
    "v_mfma_f32_16x16x32_bf16 v[124:127], a[132:135], a[12:15], 0\n"
    "v_exp_f32 v114, v114\n"
    "v_mfma_f32_16x16x32_bf16 v[124:127], a[136:139], a[16:19], v[124:127]\n"
    "v_exp_f32 v115, v115\n"
    "v_mfma_f32_16x16x32_bf16 v[124:127], a[140:143], a[20:23], v[124:127]\n"
    "v_exp_f32 v116, v116\n"
    "v_smfmac_f32_16x16x64_bf16 v[24:27], a[240:243], v[152:159], v239\n"
    "ds_read_b128 a[72:75], v171 offset:0\n"
    "ds_read_b128 a[76:79], v171 offset:64\n"
    "v_mfma_f32_16x16x32_bf16 v[0:3], a[192:195], v[152:155], v[0:3]\n"
    "v_exp_f32 v117, v117\n"
    "v_mfma_f32_16x16x32_bf16 v[0:3], a[216:219], v[156:159], v[0:3]\n"
    "v_exp_f32 v118, v118\n"
    "v_mfma_f32_16x16x32_bf16 v[4:7], a[196:199], v[152:155], v[4:7]\n"
    "v_exp_f32 v119, v119\n"
    "v_mfma_f32_16x16x32_bf16 v[4:7], a[220:223], v[156:159], v[4:7]\n"
    "v_exp_f32 v120, v120\n"
    "v_mfma_f32_16x16x32_bf16 v[8:11], a[200:203], v[152:155], v[8:11]\n"
    "ds_read_b128 a[80:83], v171 offset:128\n"
    "ds_read_b128 a[84:87], v171 offset:3072\n"
    "v_mfma_f32_16x16x32_bf16 v[8:11], a[224:227], v[156:159], v[8:11]\n"
    "v_exp_f32 v121, v121\n"
    "v_mfma_f32_16x16x32_bf16 v[12:15], a[204:207], v[152:155], v[12:15]\n"
    "v_exp_f32 v122, v122\n"
    "v_mfma_f32_16x16x32_bf16 v[12:15], a[228:231], v[156:159], v[12:15]\n"
    "ds_read_b128 a[88:91], v171 offset:3136\n"
    "v_exp_f32 v123, v123\n"
    "v_mfma_f32_16x16x32_bf16 v[16:19], a[208:211], v[152:155], v[16:19]\n"
    "v_exp_f32 v124, v124\n"
    "v_mfma_f32_16x16x32_bf16 v[16:19], a[232:235], v[156:159], v[16:19]\n"
    "v_exp_f32 v125, v125\n"
    "v_mfma_f32_16x16x32_bf16 v[20:23], a[212:215], v[152:155], v[20:23]\n"
    "ds_read_b128 a[92:95], v171 offset:3200\n"
    "v_exp_f32 v126, v126\n"
    "v_mfma_f32_16x16x32_bf16 v[20:23], a[236:239], v[156:159], v[20:23]\n"
    "v_exp_f32 v127, v127\n"
    "v_mfma_f32_16x16x32_bf16 v[128:131], a[96:99], a[24:27], 0\n"
    "ds_read_b64_tr_b16 a[144:145], v169 offset:0\n"
    "v_cvt_pk_bf16_f32 v152, v112, v113\n"
    "v_mfma_f32_16x16x32_bf16 v[128:131], a[100:103], a[28:31], v[128:131]\n"
    "ds_read_b64_tr_b16 a[146:147], v169 offset:3072\n"
    "v_cvt_pk_bf16_f32 v153, v114, v115\n"
    "v_mfma_f32_16x16x32_bf16 v[128:131], a[104:107], a[32:35], v[128:131]\n"
    "ds_read_b64_tr_b16 a[168:169], v172 offset:0\n"
    "v_cvt_pk_bf16_f32 v154, v116, v117\n"
    "v_mfma_f32_16x16x32_bf16 v[132:135], a[108:111], a[24:27], 0\n"
    "ds_read_b64_tr_b16 a[170:171], v172 offset:3072\n"
    "v_cvt_pk_bf16_f32 v155, v118, v119\n"
    "v_mfma_f32_16x16x32_bf16 v[132:135], a[112:115], a[28:31], v[132:135]\n"
    "ds_read_b64_tr_b16 a[148:149], v170 offset:0\n"
    "v_cvt_pk_bf16_f32 v156, v120, v121\n"
    "v_mfma_f32_16x16x32_bf16 v[132:135], a[116:119], a[32:35], v[132:135]\n"
    "ds_read_b64_tr_b16 a[150:151], v170 offset:3072\n"
    "v_cvt_pk_bf16_f32 v157, v122, v123\n"
    "v_mfma_f32_16x16x32_bf16 v[136:139], a[120:123], a[24:27], 0\n"
    "ds_read_b64_tr_b16 a[172:173], v173 offset:0\n"
    "v_cvt_pk_bf16_f32 v158, v124, v125\n"
    "v_mfma_f32_16x16x32_bf16 v[136:139], a[124:127], a[28:31], v[136:139]\n"
    "ds_read_b64_tr_b16 a[174:175], v173 offset:3072\n"
    "v_cvt_pk_bf16_f32 v159, v126, v127\n"
    "v_mfma_f32_16x16x32_bf16 v[136:139], a[128:131], a[32:35], v[136:139]\n"
    "v_exp_f32 v128, v128\n"
    "v_mfma_f32_16x16x32_bf16 v[140:143], a[132:135], a[24:27], 0\n"
    "v_exp_f32 v129, v129\n"
    "v_mfma_f32_16x16x32_bf16 v[140:143], a[136:139], a[28:31], v[140:143]\n"
    "global_load_dwordx2 v[224:225], v230, s[84:85] sc1\n"
    "v_exp_f32 v130, v130\n"
    "v_mfma_f32_16x16x32_bf16 v[140:143], a[140:143], a[32:35], v[140:143]\n"
    "v_exp_f32 v131, v131\n"
    "v_mfma_f32_16x16x32_bf16 v[144:147], a[120:123], a[36:39], v[164:167]\n"
    "v_exp_f32 v132, v132\n"
    "v_mfma_f32_16x16x32_bf16 v[144:147], a[124:127], a[40:43], v[144:147]\n"
    "v_exp_f32 v133, v133\n"
    "v_mfma_f32_16x16x32_bf16 v[144:147], a[128:131], a[44:47], v[144:147]\n"
    "v_exp_f32 v134, v134\n"
    "v_mfma_f32_16x16x32_bf16 v[148:151], a[132:135], a[36:39], v[164:167]\n"
    "v_exp_f32 v135, v135\n"
    "v_mfma_f32_16x16x32_bf16 v[148:151], a[136:139], a[40:43], v[148:151]\n"
    "v_exp_f32 v136, v136\n"
    "v_mfma_f32_16x16x32_bf16 v[148:151], a[140:143], a[44:47], v[148:151]\n"
    "v_exp_f32 v137, v137\n"
    "v_smfmac_f32_16x16x64_bf16 v[52:55], a[240:243], v[152:159], v239\n"
    "ds_read_b64_tr_b16 a[152:153], v169 offset:64\n"
    "ds_read_b64_tr_b16 a[154:155], v169 offset:3136\n"
    "v_mfma_f32_16x16x32_bf16 v[28:31], a[192:195], v[152:155], v[28:31]\n"
    "v_exp_f32 v138, v138\n"
    "v_mfma_f32_16x16x32_bf16 v[28:31], a[216:219], v[156:159], v[28:31]\n"
    "v_exp_f32 v139, v139\n"
    "v_mfma_f32_16x16x32_bf16 v[32:35], a[196:199], v[152:155], v[32:35]\n"
    "v_exp_f32 v140, v140\n"
    "v_mfma_f32_16x16x32_bf16 v[32:35], a[220:223], v[156:159], v[32:35]\n"
    "v_exp_f32 v141, v141\n"
    "v_mfma_f32_16x16x32_bf16 v[36:39], a[200:203], v[152:155], v[36:39]\n"
    "v_exp_f32 v142, v142\n"
    "v_mfma_f32_16x16x32_bf16 v[36:39], a[224:227], v[156:159], v[36:39]\n"
    "v_exp_f32 v143, v143\n"
    "v_mfma_f32_16x16x32_bf16 v[40:43], a[204:207], v[152:155], v[40:43]\n"
    "v_exp_f32 v144, v144\n"
    "v_mfma_f32_16x16x32_bf16 v[40:43], a[228:231], v[156:159], v[40:43]\n"
    "v_exp_f32 v145, v145\n"
    "v_mfma_f32_16x16x32_bf16 v[44:47], a[208:211], v[152:155], v[44:47]\n"
    "v_exp_f32 v146, v146\n"
    "v_mfma_f32_16x16x32_bf16 v[44:47], a[232:235], v[156:159], v[44:47]\n"
    "v_exp_f32 v147, v147\n"
    "v_mfma_f32_16x16x32_bf16 v[48:51], a[212:215], v[152:155], v[48:51]\n"
    "v_exp_f32 v148, v148\n"
    "v_mfma_f32_16x16x32_bf16 v[48:51], a[236:239], v[156:159], v[48:51]\n"
    "s_waitcnt lgkmcnt(0)\n"
    "v_exp_f32 v149, v149\n"
    "v_mfma_f32_16x16x32_bf16 v[112:115], a[48:51], a[0:3], 0\n"
    "ds_read_b64_tr_b16 a[176:177], v172 offset:64\n"
    "v_cvt_pk_bf16_f32 v152, v128, v129\n"
    "v_mfma_f32_16x16x32_bf16 v[112:115], a[52:55], a[4:7], v[112:115]\n"
    "ds_read_b64_tr_b16 a[178:179], v172 offset:3136\n"
    "v_cvt_pk_bf16_f32 v153, v130, v131\n"
    "v_mfma_f32_16x16x32_bf16 v[112:115], a[56:59], a[8:11], v[112:115]\n"
    "ds_read_b64_tr_b16 a[156:157], v170 offset:64\n"
    "v_cvt_pk_bf16_f32 v154, v132, v133\n"
    "v_mfma_f32_16x16x32_bf16 v[116:119], a[60:63], a[0:3], 0\n"
    "ds_read_b64_tr_b16 a[158:159], v170 offset:3136\n"
    "v_cvt_pk_bf16_f32 v155, v134, v135\n"
    "v_mfma_f32_16x16x32_bf16 v[116:119], a[64:67], a[4:7], v[116:119]\n"
    "ds_read_b64_tr_b16 a[180:181], v173 offset:64\n"
    "v_cvt_pk_bf16_f32 v156, v136, v137\n"
    "v_mfma_f32_16x16x32_bf16 v[116:119], a[68:71], a[8:11], v[116:119]\n"
    "ds_read_b64_tr_b16 a[182:183], v173 offset:3136\n"
    "v_cvt_pk_bf16_f32 v157, v138, v139\n"
    "v_mfma_f32_16x16x32_bf16 v[120:123], a[72:75], a[0:3], 0\n"
    "ds_read_b64_tr_b16 a[160:161], v169 offset:128\n"
    "v_cvt_pk_bf16_f32 v158, v140, v141\n"
    "v_mfma_f32_16x16x32_bf16 v[120:123], a[76:79], a[4:7], v[120:123]\n"
    "ds_read_b64_tr_b16 a[162:163], v169 offset:3200\n"
    "v_cvt_pk_bf16_f32 v159, v142, v143\n"
    "v_mfma_f32_16x16x32_bf16 v[120:123], a[80:83], a[8:11], v[120:123]\n"
    "v_exp_f32 v150, v150\n"
    "v_mfma_f32_16x16x32_bf16 v[124:127], a[84:87], a[0:3], 0\n"
    "v_exp_f32 v151, v151\n"
    "v_mfma_f32_16x16x32_bf16 v[124:127], a[88:91], a[4:7], v[124:127]\n"
    "ds_read_b64_tr_b16 a[184:185], v172 offset:128\n"
    "v_cvt_pk_bf16_f32 v160, v144, v145\n"
    "v_mfma_f32_16x16x32_bf16 v[124:127], a[92:95], a[8:11], v[124:127]\n"
    "ds_read_b64_tr_b16 a[186:187], v172 offset:3200\n"
    "v_cvt_pk_bf16_f32 v161, v146, v147\n"
    "v_smfmac_f32_16x16x64_bf16 v[80:83], a[240:243], v[152:159], v239\n"
    "ds_read_b64_tr_b16 a[164:165], v170 offset:128\n"
    "v_cvt_pk_bf16_f32 v162, v148, v149\n"
    "v_mfma_f32_16x16x32_bf16 v[56:59], a[192:195], v[152:155], v[56:59]\n"
    "ds_read_b64_tr_b16 a[166:167], v170 offset:3200\n"
    "v_cvt_pk_bf16_f32 v163, v150, v151\n"
    "v_mfma_f32_16x16x32_bf16 v[56:59], a[216:219], v[156:159], v[56:59]\n"
    "s_add_u32 m0, s40, 52224\n"
    "v_exp_f32 v112, v112\n"
    "v_mfma_f32_16x16x32_bf16 v[60:63], a[196:199], v[152:155], v[60:63]\n"
    "global_load_lds_dwordx4 v216, s[54:55] offset:-3072 sc1\n"
    "v_exp_f32 v113, v113\n"
    "v_mfma_f32_16x16x32_bf16 v[60:63], a[220:223], v[156:159], v[60:63]\n"
    "v_exp_f32 v114, v114\n"
    "v_mfma_f32_16x16x32_bf16 v[64:67], a[200:203], v[152:155], v[64:67]\n"
    "ds_read_b64_tr_b16 a[188:189], v173 offset:128\n"
    "global_load_lds_dwordx4 v216, s[54:55] offset:-2048 sc1\n"
    "v_mfma_f32_16x16x32_bf16 v[64:67], a[224:227], v[156:159], v[64:67]\n"
    "v_exp_f32 v115, v115\n"
    "v_mfma_f32_16x16x32_bf16 v[68:71], a[204:207], v[152:155], v[68:71]\n"
    "v_exp_f32 v116, v116\n"
    "v_mfma_f32_16x16x32_bf16 v[68:71], a[228:231], v[156:159], v[68:71]\n"
    "global_load_lds_dwordx4 v216, s[54:55] offset:-1024 sc1\n"
    "v_exp_f32 v117, v117\n"
    "v_mfma_f32_16x16x32_bf16 v[72:75], a[208:211], v[152:155], v[72:75]\n"
    "v_exp_f32 v118, v118\n"
    "v_mfma_f32_16x16x32_bf16 v[72:75], a[232:235], v[156:159], v[72:75]\n"
    "v_exp_f32 v119, v119\n"
    "v_mfma_f32_16x16x32_bf16 v[76:79], a[212:215], v[152:155], v[76:79]\n"
    "ds_read_b64_tr_b16 a[190:191], v173 offset:3200\n"
    "global_load_lds_dwordx4 v216, s[54:55] offset:0 sc1\n"
    "v_mfma_f32_16x16x32_bf16 v[76:79], a[236:239], v[156:159], v[76:79]\n"
    "v_exp_f32 v120, v120\n"
    "v_mfma_f32_16x16x32_bf16 v[84:87], a[216:219], v[160:163], v[84:87]\n"
    "v_exp_f32 v121, v121\n"
    "v_mfma_f32_16x16x32_bf16 v[88:91], a[220:223], v[160:163], v[88:91]\n"
    "global_load_lds_dwordx4 v216, s[54:55] offset:1024 sc1\n"
    "v_exp_f32 v122, v122\n"
    "v_mfma_f32_16x16x32_bf16 v[92:95], a[224:227], v[160:163], v[92:95]\n"
    "v_exp_f32 v123, v123\n"
    "v_mfma_f32_16x16x32_bf16 v[96:99], a[228:231], v[160:163], v[96:99]\n"
    "v_exp_f32 v124, v124\n"
    "v_mfma_f32_16x16x32_bf16 v[100:103], a[232:235], v[160:163], v[100:103]\n"
    "global_load_lds_dwordx4 v216, s[54:55] offset:2048 sc1\n"
    "v_exp_f32 v125, v125\n"
    "v_mfma_f32_16x16x32_bf16 v[104:107], a[236:239], v[160:163], v[104:107]\n"
    "v_exp_f32 v126, v126\n"
    "v_mfma_f32_16x16x32_bf16 v[108:111], a[240:243], v[160:163], v[108:111]\n"
    "v_exp_f32 v127, v127\n"
    "v_mfma_f32_16x16x32_bf16 v[128:131], a[48:51], a[12:15], 0\n"
    "ds_read_b128 a[96:99], v174 offset:0\n"
    "v_cvt_pk_bf16_f32 v152, v112, v113\n"
    "v_mfma_f32_16x16x32_bf16 v[128:131], a[52:55], a[16:19], v[128:131]\n"
    "ds_read_b128 a[100:103], v174 offset:64\n"
    "v_cvt_pk_bf16_f32 v153, v114, v115\n"
    "v_mfma_f32_16x16x32_bf16 v[128:131], a[56:59], a[20:23], v[128:131]\n"
    "ds_read_b128 a[104:107], v174 offset:128\n"
    "v_cvt_pk_bf16_f32 v154, v116, v117\n"
    "v_mfma_f32_16x16x32_bf16 v[132:135], a[60:63], a[12:15], 0\n"
    "ds_read_b128 a[108:111], v174 offset:3072\n"
    "v_cvt_pk_bf16_f32 v155, v118, v119\n"
    "v_mfma_f32_16x16x32_bf16 v[132:135], a[64:67], a[16:19], v[132:135]\n"
    "ds_read_b128 a[112:115], v174 offset:3136\n"
    "v_cvt_pk_bf16_f32 v156, v120, v121\n"
    "s_nop 0\n"
    "twins_p4:\n"
    "s_cbranch_execz twins_ret\n"
    "v_mfma_f32_16x16x32_bf16 v[132:135], a[68:71], a[20:23], v[132:135]\n"
    "ds_read_b128 a[116:119], v174 offset:3200\n"
    "v_cvt_pk_bf16_f32 v157, v122, v123\n"
    "v_mfma_f32_16x16x32_bf16 v[136:139], a[72:75], a[12:15], 0\n"
    "ds_read_b128 a[120:123], v177 offset:0\n"
    "v_cvt_pk_bf16_f32 v158, v124, v125\n"
    "v_mfma_f32_16x16x32_bf16 v[136:139], a[76:79], a[16:19], v[136:139]\n"
    "ds_read_b128 a[124:127], v177 offset:64\n"
    "v_cvt_pk_bf16_f32 v159, v126, v127\n"
    "v_mfma_f32_16x16x32_bf16 v[136:139], a[80:83], a[20:23], v[136:139]\n"
    "s_add_u32 m0, s63, 100352\n"
    "v_exp_f32 v128, v128\n"
    "v_mfma_f32_16x16x32_bf16 v[140:143], a[84:87], a[12:15], 0\n"
    "global_load_lds_dwordx4 v216, s[76:77] offset:-2048 sc1\n"
    "v_exp_f32 v129, v129\n"
    "v_mfma_f32_16x16x32_bf16 v[140:143], a[88:91], a[16:19], v[140:143]\n"
    "v_exp_f32 v130, v130\n"
    "v_mfma_f32_16x16x32_bf16 v[140:143], a[92:95], a[20:23], v[140:143]\n"
    "v_exp_f32 v131, v131\n"
    "v_smfmac_f32_16x16x64_bf16 v[24:27], a[240:243], v[152:159], v239\n"
    "ds_read_b128 a[128:131], v177 offset:128\n"
    "ds_read_b128 a[132:135], v177 offset:3072\n"
    "v_mfma_f32_16x16x32_bf16 v[0:3], a[144:147], v[152:155], v[0:3]\n"
    "global_load_lds_dwordx4 v216, s[76:77] offset:-1024 sc1\n"
    "v_exp_f32 v132, v132\n"
    "v_mfma_f32_16x16x32_bf16 v[0:3], a[168:171], v[156:159], v[0:3]\n"
    "v_exp_f32 v133, v133\n"
    "v_mfma_f32_16x16x32_bf16 v[4:7], a[148:151], v[152:155], v[4:7]\n"
    "s_waitcnt lgkmcnt(2)\n"
    "v_exp_f32 v134, v134\n"
    "v_mfma_f32_16x16x32_bf16 v[4:7], a[172:175], v[156:159], v[4:7]\n"
    "ds_read_b128 a[136:139], v177 offset:3136\n"
    "v_exp_f32 v135, v135\n"
    "v_mfma_f32_16x16x32_bf16 v[8:11], a[152:155], v[152:155], v[8:11]\n"
    "global_load_lds_dwordx4 v216, s[76:77] offset:0 sc1\n"
    "v_exp_f32 v136, v136\n"
    "v_mfma_f32_16x16x32_bf16 v[8:11], a[176:179], v[156:159], v[8:11]\n"
    "v_exp_f32 v137, v137\n"
    "v_mfma_f32_16x16x32_bf16 v[12:15], a[156:159], v[152:155], v[12:15]\n"
    "v_exp_f32 v138, v138\n"
    "v_mfma_f32_16x16x32_bf16 v[12:15], a[180:183], v[156:159], v[12:15]\n"
    "v_exp_f32 v139, v139\n"
    "v_mfma_f32_16x16x32_bf16 v[16:19], a[160:163], v[152:155], v[16:19]\n"
    "ds_read_b128 a[140:143], v177 offset:3200\n"
    "v_exp_f32 v140, v140\n"
    "v_mfma_f32_16x16x32_bf16 v[16:19], a[184:187], v[156:159], v[16:19]\n"
    "v_exp_f32 v141, v141\n"
    "v_mfma_f32_16x16x32_bf16 v[20:23], a[164:167], v[152:155], v[20:23]\n"
    "v_exp_f32 v142, v142\n"
    "v_mfma_f32_16x16x32_bf16 v[20:23], a[188:191], v[156:159], v[20:23]\n"
    "v_exp_f32 v143, v143\n"
    "v_mfma_f32_16x16x32_bf16 v[112:115], a[48:51], a[24:27], 0\n"
    "ds_read_b64_tr_b16 a[192:193], v175 offset:0\n"
    "v_cvt_pk_bf16_f32 v152, v128, v129\n"
    "v_mfma_f32_16x16x32_bf16 v[112:115], a[52:55], a[28:31], v[112:115]\n"
    "ds_read_b64_tr_b16 a[194:195], v175 offset:3072\n"
    "v_cvt_pk_bf16_f32 v153, v130, v131\n"
    "v_mfma_f32_16x16x32_bf16 v[112:115], a[56:59], a[32:35], v[112:115]\n"
    "ds_read_b64_tr_b16 a[216:217], v178 offset:0\n"
    "v_cvt_pk_bf16_f32 v154, v132, v133\n"
    "v_mfma_f32_16x16x32_bf16 v[116:119], a[60:63], a[24:27], 0\n"
    "ds_read_b64_tr_b16 a[218:219], v178 offset:3072\n"
    "v_cvt_pk_bf16_f32 v155, v134, v135\n"
    "v_mfma_f32_16x16x32_bf16 v[116:119], a[64:67], a[28:31], v[116:119]\n"
    "ds_read_b64_tr_b16 a[196:197], v176 offset:0\n"
    "v_cvt_pk_bf16_f32 v156, v136, v137\n"
    "v_mfma_f32_16x16x32_bf16 v[116:119], a[68:71], a[32:35], v[116:119]\n"
    "ds_read_b64_tr_b16 a[198:199], v176 offset:3072\n"
    "v_cvt_pk_bf16_f32 v157, v138, v139\n"
    "v_mfma_f32_16x16x32_bf16 v[120:123], a[72:75], a[24:27], 0\n"
    "ds_read_b64_tr_b16 a[220:221], v179 offset:0\n"
    "v_cvt_pk_bf16_f32 v158, v140, v141\n"
    "v_mfma_f32_16x16x32_bf16 v[120:123], a[76:79], a[28:31], v[120:123]\n"
    "ds_read_b64_tr_b16 a[222:223], v179 offset:3072\n"
    "v_cvt_pk_bf16_f32 v159, v142, v143\n"
    "v_mfma_f32_16x16x32_bf16 v[120:123], a[80:83], a[32:35], v[120:123]\n"
    "global_load_lds_dwordx4 v216, s[76:77] offset:1024 sc1\n"
    "v_exp_f32 v112, v112\n"
    "v_mfma_f32_16x16x32_bf16 v[124:127], a[84:87], a[24:27], 0\n"
    "v_exp_f32 v113, v113\n"
    "v_mfma_f32_16x16x32_bf16 v[124:127], a[88:91], a[28:31], v[124:127]\n"
    "v_exp_f32 v114, v114\n"
    "v_mfma_f32_16x16x32_bf16 v[124:127], a[92:95], a[32:35], v[124:127]\n"
    "v_exp_f32 v115, v115\n"
    "v_smfmac_f32_16x16x64_bf16 v[52:55], a[240:243], v[152:159], v239\n"
    "ds_read_b64_tr_b16 a[200:201], v175 offset:64\n"
    "ds_read_b64_tr_b16 a[202:203], v175 offset:3136\n"
    "v_mfma_f32_16x16x32_bf16 v[28:31], a[144:147], v[152:155], v[28:31]\n"
    "global_load_lds_dwordx4 v216, s[76:77] offset:2048 sc1\n"
    "v_exp_f32 v116, v116\n"
    "v_mfma_f32_16x16x32_bf16 v[28:31], a[168:171], v[156:159], v[28:31]\n"
    "v_exp_f32 v117, v117\n"
    "v_mfma_f32_16x16x32_bf16 v[32:35], a[148:151], v[152:155], v[32:35]\n"
    "v_exp_f32 v118, v118\n"
    "v_mfma_f32_16x16x32_bf16 v[32:35], a[172:175], v[156:159], v[32:35]\n"
    "ds_read_b64_tr_b16 a[224:225], v178 offset:64\n"
    "v_exp_f32 v119, v119\n"
    "v_mfma_f32_16x16x32_bf16 v[36:39], a[152:155], v[152:155], v[36:39]\n"
    "v_exp_f32 v120, v120\n"
    "v_mfma_f32_16x16x32_bf16 v[36:39], a[176:179], v[156:159], v[36:39]\n"
    "v_exp_f32 v121, v121\n"
    "v_mfma_f32_16x16x32_bf16 v[40:43], a[156:159], v[152:155], v[40:43]\n"
    "v_exp_f32 v122, v122\n"
    "v_mfma_f32_16x16x32_bf16 v[40:43], a[180:183], v[156:159], v[40:43]\n"
    "v_exp_f32 v123, v123\n"
    "v_mfma_f32_16x16x32_bf16 v[44:47], a[160:163], v[152:155], v[44:47]\n"
    "ds_read_b64_tr_b16 a[226:227], v178 offset:3136\n"
    "v_exp_f32 v124, v124\n"
    "v_mfma_f32_16x16x32_bf16 v[44:47], a[184:187], v[156:159], v[44:47]\n"
    "v_exp_f32 v125, v125\n"
    "v_mfma_f32_16x16x32_bf16 v[48:51], a[164:167], v[152:155], v[48:51]\n"
    "v_exp_f32 v126, v126\n"
    "v_mfma_f32_16x16x32_bf16 v[48:51], a[188:191], v[156:159], v[48:51]\n"
    "v_exp_f32 v127, v127\n"
    "v_mfma_f32_16x16x32_bf16 v[128:131], a[96:99], a[0:3], 0\n"
    "ds_read_b64_tr_b16 a[204:205], v176 offset:64\n"
    "v_cvt_pk_bf16_f32 v152, v112, v113\n"
    "v_mfma_f32_16x16x32_bf16 v[128:131], a[100:103], a[4:7], v[128:131]\n"
    "ds_read_b64_tr_b16 a[206:207], v176 offset:3136\n"
    "v_cvt_pk_bf16_f32 v153, v114, v115\n"
    "v_mfma_f32_16x16x32_bf16 v[128:131], a[104:107], a[8:11], v[128:131]\n"
    "ds_read_b64_tr_b16 a[228:229], v179 offset:64\n"
    "v_cvt_pk_bf16_f32 v154, v116, v117\n"
    "v_mfma_f32_16x16x32_bf16 v[132:135], a[108:111], a[0:3], 0\n"
    "ds_read_b64_tr_b16 a[230:231], v179 offset:3136\n"
    "v_cvt_pk_bf16_f32 v155, v118, v119\n"
    "v_mfma_f32_16x16x32_bf16 v[132:135], a[112:115], a[4:7], v[132:135]\n"
    "ds_read_b64_tr_b16 a[208:209], v175 offset:128\n"
    "v_cvt_pk_bf16_f32 v156, v120, v121\n"
    "v_mfma_f32_16x16x32_bf16 v[132:135], a[116:119], a[8:11], v[132:135]\n"
    "ds_read_b64_tr_b16 a[210:211], v175 offset:3200\n"
    "v_cvt_pk_bf16_f32 v157, v122, v123\n"
    "v_mfma_f32_16x16x32_bf16 v[136:139], a[120:123], a[0:3], 0\n"
    "ds_read_b64_tr_b16 a[232:233], v178 offset:128\n"
    "v_cvt_pk_bf16_f32 v158, v124, v125\n"
    "v_mfma_f32_16x16x32_bf16 v[136:139], a[124:127], a[4:7], v[136:139]\n"
    "s_waitcnt lgkmcnt(6)\n"
    "ds_read_b64_tr_b16 a[234:235], v178 offset:3200\n"
    "v_cvt_pk_bf16_f32 v159, v126, v127\n"
    "v_mfma_f32_16x16x32_bf16 v[136:139], a[128:131], a[8:11], v[136:139]\n"
    "v_exp_f32 v128, v128\n"
    "v_mfma_f32_16x16x32_bf16 v[140:143], a[132:135], a[0:3], 0\n"
    "v_exp_f32 v129, v129\n"
    "v_mfma_f32_16x16x32_bf16 v[140:143], a[136:139], a[4:7], v[140:143]\n"
    "v_exp_f32 v130, v130\n"
    "v_mfma_f32_16x16x32_bf16 v[140:143], a[140:143], a[8:11], v[140:143]\n"
    "v_exp_f32 v131, v131\n"
    "v_smfmac_f32_16x16x64_bf16 v[80:83], a[240:243], v[152:159], v239\n"
    "ds_read_b64_tr_b16 a[212:213], v176 offset:128\n"
    "ds_read_b64_tr_b16 a[214:215], v176 offset:3200\n"
    "v_mfma_f32_16x16x32_bf16 v[56:59], a[144:147], v[152:155], v[56:59]\n"
    "v_exp_f32 v132, v132\n"
    "v_mfma_f32_16x16x32_bf16 v[56:59], a[168:171], v[156:159], v[56:59]\n"
    "v_exp_f32 v133, v133\n"
    "v_mfma_f32_16x16x32_bf16 v[60:63], a[148:151], v[152:155], v[60:63]\n"
    "v_exp_f32 v134, v134\n"
    "v_mfma_f32_16x16x32_bf16 v[60:63], a[172:175], v[156:159], v[60:63]\n"
    "ds_read_b64_tr_b16 a[236:237], v179 offset:128\n"
    "v_exp_f32 v135, v135\n"
    "v_mfma_f32_16x16x32_bf16 v[64:67], a[152:155], v[152:155], v[64:67]\n"
    "v_exp_f32 v136, v136\n"
    "v_mfma_f32_16x16x32_bf16 v[64:67], a[176:179], v[156:159], v[64:67]\n"
    "v_exp_f32 v137, v137\n"
    "v_mfma_f32_16x16x32_bf16 v[68:71], a[156:159], v[152:155], v[68:71]\n"
    "v_exp_f32 v138, v138\n"
    "v_mfma_f32_16x16x32_bf16 v[68:71], a[180:183], v[156:159], v[68:71]\n"
    "v_exp_f32 v139, v139\n"
    "v_mfma_f32_16x16x32_bf16 v[72:75], a[160:163], v[152:155], v[72:75]\n"
    "ds_read_b64_tr_b16 a[238:239], v179 offset:3200\n"
    "v_exp_f32 v140, v140\n"
    "v_mfma_f32_16x16x32_bf16 v[72:75], a[184:187], v[156:159], v[72:75]\n"
    "v_exp_f32 v141, v141\n"
    "v_mfma_f32_16x16x32_bf16 v[76:79], a[164:167], v[152:155], v[76:79]\n"
    "v_exp_f32 v142, v142\n"
    "v_mfma_f32_16x16x32_bf16 v[76:79], a[188:191], v[156:159], v[76:79]\n"
    "v_exp_f32 v143, v143\n"
    "v_mfma_f32_16x16x32_bf16 v[112:115], a[96:99], a[12:15], 0\n"
    "ds_read_b128 a[48:51], v168 offset:24576\n"
    "v_cvt_pk_bf16_f32 v152, v128, v129\n"
    "v_mfma_f32_16x16x32_bf16 v[112:115], a[100:103], a[16:19], v[112:115]\n"
    "ds_read_b128 a[52:55], v168 offset:24640\n"
    "v_cvt_pk_bf16_f32 v153, v130, v131\n"
    "v_mfma_f32_16x16x32_bf16 v[112:115], a[104:107], a[20:23], v[112:115]\n"
    "ds_read_b128 a[56:59], v168 offset:24704\n"
    "v_cvt_pk_bf16_f32 v154, v132, v133\n"
    "v_mfma_f32_16x16x32_bf16 v[116:119], a[108:111], a[12:15], 0\n"
    "ds_read_b128 a[60:63], v168 offset:27648\n"
    "v_cvt_pk_bf16_f32 v155, v134, v135\n"
    "v_mfma_f32_16x16x32_bf16 v[116:119], a[112:115], a[16:19], v[116:119]\n"
    "ds_read_b128 a[64:67], v168 offset:27712\n"
    "v_cvt_pk_bf16_f32 v156, v136, v137\n"
    "v_mfma_f32_16x16x32_bf16 v[116:119], a[116:119], a[20:23], v[116:119]\n"
    "ds_read_b128 a[68:71], v168 offset:27776\n"
    "v_cvt_pk_bf16_f32 v157, v138, v139\n"
    "v_mfma_f32_16x16x32_bf16 v[120:123], a[120:123], a[12:15], 0\n"
    "ds_read_b128 a[72:75], v171 offset:24576\n"
    "v_cvt_pk_bf16_f32 v158, v140, v141\n"
    "v_mfma_f32_16x16x32_bf16 v[120:123], a[124:127], a[16:19], v[120:123]\n"
    "ds_read_b128 a[76:79], v171 offset:24640\n"
    "v_cvt_pk_bf16_f32 v159, v142, v143\n"
    "v_mfma_f32_16x16x32_bf16 v[120:123], a[128:131], a[20:23], v[120:123]\n"
    "v_exp_f32 v112, v112\n"
    "v_mfma_f32_16x16x32_bf16 v[124:127], a[132:135], a[12:15], 0\n"
    "v_exp_f32 v113, v113\n"
    "v_mfma_f32_16x16x32_bf16 v[124:127], a[136:139], a[16:19], v[124:127]\n"
    "v_exp_f32 v114, v114\n"
    "v_mfma_f32_16x16x32_bf16 v[124:127], a[140:143], a[20:23], v[124:127]\n"
    "v_exp_f32 v115, v115\n"
    "v_smfmac_f32_16x16x64_bf16 v[24:27], a[240:243], v[152:159], v239\n"
    "ds_read_b128 a[80:83], v171 offset:24704\n"
    "ds_read_b128 a[84:87], v171 offset:27648\n"
    "v_mfma_f32_16x16x32_bf16 v[0:3], a[192:195], v[152:155], v[0:3]\n"
    "v_exp_f32 v116, v116\n"
    "v_mfma_f32_16x16x32_bf16 v[0:3], a[216:219], v[156:159], v[0:3]\n"
    "v_exp_f32 v117, v117\n"
    "v_mfma_f32_16x16x32_bf16 v[4:7], a[196:199], v[152:155], v[4:7]\n"
    "v_exp_f32 v118, v118\n"
    "v_mfma_f32_16x16x32_bf16 v[4:7], a[220:223], v[156:159], v[4:7]\n"
    "ds_read_b128 a[88:91], v171 offset:27712\n"
    "v_exp_f32 v119, v119\n"
    "v_mfma_f32_16x16x32_bf16 v[8:11], a[200:203], v[152:155], v[8:11]\n"
    "v_exp_f32 v120, v120\n"
    "v_mfma_f32_16x16x32_bf16 v[8:11], a[224:227], v[156:159], v[8:11]\n"
    "s_waitcnt lgkmcnt(3)\n"
    "v_exp_f32 v121, v121\n"
    "v_mfma_f32_16x16x32_bf16 v[12:15], a[204:207], v[152:155], v[12:15]\n"
    "v_exp_f32 v122, v122\n"
    "v_mfma_f32_16x16x32_bf16 v[12:15], a[228:231], v[156:159], v[12:15]\n"
    "v_exp_f32 v123, v123\n"
    "v_mfma_f32_16x16x32_bf16 v[16:19], a[208:211], v[152:155], v[16:19]\n"
    "ds_read_b128 a[92:95], v171 offset:27776\n"
    "v_exp_f32 v124, v124\n"
    "v_mfma_f32_16x16x32_bf16 v[16:19], a[232:235], v[156:159], v[16:19]\n"
    "v_exp_f32 v125, v125\n"
    "v_mfma_f32_16x16x32_bf16 v[20:23], a[212:215], v[152:155], v[20:23]\n"
    "v_exp_f32 v126, v126\n"
    "v_mfma_f32_16x16x32_bf16 v[20:23], a[236:239], v[156:159], v[20:23]\n"
    "v_exp_f32 v127, v127\n"
    "v_mfma_f32_16x16x32_bf16 v[128:131], a[96:99], a[24:27], 0\n"
    "ds_read_b64_tr_b16 a[144:145], v169 offset:24576\n"
    "v_cvt_pk_bf16_f32 v152, v112, v113\n"
    "v_mfma_f32_16x16x32_bf16 v[128:131], a[100:103], a[28:31], v[128:131]\n"
    "ds_read_b64_tr_b16 a[146:147], v169 offset:27648\n"
    "v_cvt_pk_bf16_f32 v153, v114, v115\n"
    "v_mfma_f32_16x16x32_bf16 v[128:131], a[104:107], a[32:35], v[128:131]\n"
    "ds_read_b64_tr_b16 a[168:169], v172 offset:24576\n"
    "v_cvt_pk_bf16_f32 v154, v116, v117\n"
    "v_mfma_f32_16x16x32_bf16 v[132:135], a[108:111], a[24:27], 0\n"
    "ds_read_b64_tr_b16 a[170:171], v172 offset:27648\n"
    "v_cvt_pk_bf16_f32 v155, v118, v119\n"
    "v_mfma_f32_16x16x32_bf16 v[132:135], a[112:115], a[28:31], v[132:135]\n"
    "ds_read_b64_tr_b16 a[148:149], v170 offset:24576\n"
    "v_cvt_pk_bf16_f32 v156, v120, v121\n"
    "v_mfma_f32_16x16x32_bf16 v[132:135], a[116:119], a[32:35], v[132:135]\n"
    "ds_read_b64_tr_b16 a[150:151], v170 offset:27648\n"
    "v_cvt_pk_bf16_f32 v157, v122, v123\n"
    "v_mfma_f32_16x16x32_bf16 v[136:139], a[120:123], a[24:27], 0\n"
    "ds_read_b64_tr_b16 a[172:173], v173 offset:24576\n"
    "v_cvt_pk_bf16_f32 v158, v124, v125\n"
    "v_mfma_f32_16x16x32_bf16 v[136:139], a[124:127], a[28:31], v[136:139]\n"
    "ds_read_b64_tr_b16 a[174:175], v173 offset:27648\n"
    "v_cvt_pk_bf16_f32 v159, v126, v127\n"
    "v_mfma_f32_16x16x32_bf16 v[136:139], a[128:131], a[32:35], v[136:139]\n"
    "v_exp_f32 v128, v128\n"
    "v_mfma_f32_16x16x32_bf16 v[140:143], a[132:135], a[24:27], 0\n"
    "v_exp_f32 v129, v129\n"
    "v_mfma_f32_16x16x32_bf16 v[140:143], a[136:139], a[28:31], v[140:143]\n"
    "v_exp_f32 v130, v130\n"
    "v_mfma_f32_16x16x32_bf16 v[140:143], a[140:143], a[32:35], v[140:143]\n"
    "v_exp_f32 v131, v131\n"
    "v_mfma_f32_16x16x32_bf16 v[144:147], a[120:123], a[36:39], v[164:167]\n"
    "v_exp_f32 v132, v132\n"
    "v_mfma_f32_16x16x32_bf16 v[144:147], a[124:127], a[40:43], v[144:147]\n"
    "v_exp_f32 v133, v133\n"
    "v_mfma_f32_16x16x32_bf16 v[144:147], a[128:131], a[44:47], v[144:147]\n"
    "v_exp_f32 v134, v134\n"
    "v_mfma_f32_16x16x32_bf16 v[148:151], a[132:135], a[36:39], v[164:167]\n"
    "v_exp_f32 v135, v135\n"
    "v_mfma_f32_16x16x32_bf16 v[148:151], a[136:139], a[40:43], v[148:151]\n"
    "v_exp_f32 v136, v136\n"
    "v_mfma_f32_16x16x32_bf16 v[148:151], a[140:143], a[44:47], v[148:151]\n"
    "v_exp_f32 v137, v137\n"
    "v_smfmac_f32_16x16x64_bf16 v[52:55], a[240:243], v[152:159], v239\n"
    "ds_read_b64_tr_b16 a[152:153], v169 offset:24640\n"
    "ds_read_b64_tr_b16 a[154:155], v169 offset:27712\n"
    "v_mfma_f32_16x16x32_bf16 v[28:31], a[192:195], v[152:155], v[28:31]\n"
    "v_exp_f32 v138, v138\n"
    "v_mfma_f32_16x16x32_bf16 v[28:31], a[216:219], v[156:159], v[28:31]\n"
    "v_exp_f32 v139, v139\n"
    "v_mfma_f32_16x16x32_bf16 v[32:35], a[196:199], v[152:155], v[32:35]\n"
    "v_exp_f32 v140, v140\n"
    "v_mfma_f32_16x16x32_bf16 v[32:35], a[220:223], v[156:159], v[32:35]\n"
    "v_exp_f32 v141, v141\n"
    "v_mfma_f32_16x16x32_bf16 v[36:39], a[200:203], v[152:155], v[36:39]\n"
    "v_exp_f32 v142, v142\n"
    "v_mfma_f32_16x16x32_bf16 v[36:39], a[224:227], v[156:159], v[36:39]\n"
    "v_exp_f32 v143, v143\n"
    "v_mfma_f32_16x16x32_bf16 v[40:43], a[204:207], v[152:155], v[40:43]\n"
    "v_exp_f32 v144, v144\n"
    "v_mfma_f32_16x16x32_bf16 v[40:43], a[228:231], v[156:159], v[40:43]\n"
    "v_exp_f32 v145, v145\n"
    "v_mfma_f32_16x16x32_bf16 v[44:47], a[208:211], v[152:155], v[44:47]\n"
    "v_exp_f32 v146, v146\n"
    "v_mfma_f32_16x16x32_bf16 v[44:47], a[232:235], v[156:159], v[44:47]\n"
    "v_exp_f32 v147, v147\n"
    "v_mfma_f32_16x16x32_bf16 v[48:51], a[212:215], v[152:155], v[48:51]\n"
    "v_exp_f32 v148, v148\n"
    "v_mfma_f32_16x16x32_bf16 v[48:51], a[236:239], v[156:159], v[48:51]\n"
    "v_exp_f32 v149, v149\n"
    "v_mfma_f32_16x16x32_bf16 v[112:115], a[48:51], a[0:3], 0\n"
    "ds_read_b64_tr_b16 a[176:177], v172 offset:24640\n"
    "v_cvt_pk_bf16_f32 v152, v128, v129\n"
    "v_mfma_f32_16x16x32_bf16 v[112:115], a[52:55], a[4:7], v[112:115]\n"
    "ds_read_b64_tr_b16 a[178:179], v172 offset:27712\n"
    "v_cvt_pk_bf16_f32 v153, v130, v131\n"
    "v_mfma_f32_16x16x32_bf16 v[112:115], a[56:59], a[8:11], v[112:115]\n"
    "ds_read_b64_tr_b16 a[156:157], v170 offset:24640\n"
    "v_cvt_pk_bf16_f32 v154, v132, v133\n"
    "v_mfma_f32_16x16x32_bf16 v[116:119], a[60:63], a[0:3], 0\n"
    "ds_read_b64_tr_b16 a[158:159], v170 offset:27712\n"
    "v_cvt_pk_bf16_f32 v155, v134, v135\n"
    "v_mfma_f32_16x16x32_bf16 v[116:119], a[64:67], a[4:7], v[116:119]\n"
    "ds_read_b64_tr_b16 a[180:181], v173 offset:24640\n"
    "v_cvt_pk_bf16_f32 v156, v136, v137\n"
    "v_mfma_f32_16x16x32_bf16 v[116:119], a[68:71], a[8:11], v[116:119]\n"
    "ds_read_b64_tr_b16 a[182:183], v173 offset:27712\n"
    "v_cvt_pk_bf16_f32 v157, v138, v139\n"
    "v_mfma_f32_16x16x32_bf16 v[120:123], a[72:75], a[0:3], 0\n"
    "ds_read_b64_tr_b16 a[160:161], v169 offset:24704\n"
    "v_cvt_pk_bf16_f32 v158, v140, v141\n"
    "v_mfma_f32_16x16x32_bf16 v[120:123], a[76:79], a[4:7], v[120:123]\n"
    "s_waitcnt lgkmcnt(6)\n"
    "ds_read_b64_tr_b16 a[162:163], v169 offset:27776\n"
    "v_cvt_pk_bf16_f32 v159, v142, v143\n"
    "v_mfma_f32_16x16x32_bf16 v[120:123], a[80:83], a[8:11], v[120:123]\n"
    "v_exp_f32 v150, v150\n"
    "v_mfma_f32_16x16x32_bf16 v[124:127], a[84:87], a[0:3], 0\n"
    "v_exp_f32 v151, v151\n"
    "v_mfma_f32_16x16x32_bf16 v[124:127], a[88:91], a[4:7], v[124:127]\n"
    "ds_read_b64_tr_b16 a[184:185], v172 offset:24704\n"
    "v_cvt_pk_bf16_f32 v160, v144, v145\n"
    "v_mfma_f32_16x16x32_bf16 v[124:127], a[92:95], a[8:11], v[124:127]\n"
    "ds_read_b64_tr_b16 a[186:187], v172 offset:27776\n"
    "v_cvt_pk_bf16_f32 v161, v146, v147\n"
    "v_smfmac_f32_16x16x64_bf16 v[80:83], a[240:243], v[152:159], v239\n"
    "ds_read_b64_tr_b16 a[164:165], v170 offset:24704\n"
    "v_cvt_pk_bf16_f32 v162, v148, v149\n"
    "v_mfma_f32_16x16x32_bf16 v[56:59], a[192:195], v[152:155], v[56:59]\n"
    "ds_read_b64_tr_b16 a[166:167], v170 offset:27776\n"
    "v_cvt_pk_bf16_f32 v163, v150, v151\n"
    "v_mfma_f32_16x16x32_bf16 v[56:59], a[216:219], v[156:159], v[56:59]\n"
    "s_add_u32 m0, s40, 76800\n"
    "v_exp_f32 v112, v112\n"
    "v_mfma_f32_16x16x32_bf16 v[60:63], a[196:199], v[152:155], v[60:63]\n"
    "global_load_lds_dwordx4 v216, s[56:57] offset:-3072 sc1\n"
    "v_exp_f32 v113, v113\n"
    "v_mfma_f32_16x16x32_bf16 v[60:63], a[220:223], v[156:159], v[60:63]\n"
    "v_exp_f32 v114, v114\n"
    "v_mfma_f32_16x16x32_bf16 v[64:67], a[200:203], v[152:155], v[64:67]\n"
    "ds_read_b64_tr_b16 a[188:189], v173 offset:24704\n"
    "global_load_lds_dwordx4 v216, s[56:57] offset:-2048 sc1\n"
    "v_mfma_f32_16x16x32_bf16 v[64:67], a[224:227], v[156:159], v[64:67]\n"
    "v_exp_f32 v115, v115\n"
    "v_mfma_f32_16x16x32_bf16 v[68:71], a[204:207], v[152:155], v[68:71]\n"
    "v_exp_f32 v116, v116\n"
    "v_mfma_f32_16x16x32_bf16 v[68:71], a[228:231], v[156:159], v[68:71]\n"
    "global_load_lds_dwordx4 v216, s[56:57] offset:-1024 sc1\n"
    "v_exp_f32 v117, v117\n"
    "v_mfma_f32_16x16x32_bf16 v[72:75], a[208:211], v[152:155], v[72:75]\n"
    "v_exp_f32 v118, v118\n"
    "v_mfma_f32_16x16x32_bf16 v[72:75], a[232:235], v[156:159], v[72:75]\n"
    "v_exp_f32 v119, v119\n"
    "v_mfma_f32_16x16x32_bf16 v[76:79], a[212:215], v[152:155], v[76:79]\n"
    "ds_read_b64_tr_b16 a[190:191], v173 offset:27776\n"
    "global_load_lds_dwordx4 v216, s[56:57] offset:0 sc1\n"
    "v_mfma_f32_16x16x32_bf16 v[76:79], a[236:239], v[156:159], v[76:79]\n"
    "v_exp_f32 v120, v120\n"
    "v_mfma_f32_16x16x32_bf16 v[84:87], a[216:219], v[160:163], v[84:87]\n"
    "v_exp_f32 v121, v121\n"
    "v_mfma_f32_16x16x32_bf16 v[88:91], a[220:223], v[160:163], v[88:91]\n"
    "global_load_lds_dwordx4 v216, s[56:57] offset:1024 sc1\n"
    "v_exp_f32 v122, v122\n"
    "v_mfma_f32_16x16x32_bf16 v[92:95], a[224:227], v[160:163], v[92:95]\n"
    "v_exp_f32 v123, v123\n"
    "v_mfma_f32_16x16x32_bf16 v[96:99], a[228:231], v[160:163], v[96:99]\n"
    "v_exp_f32 v124, v124\n"
    "v_mfma_f32_16x16x32_bf16 v[100:103], a[232:235], v[160:163], v[100:103]\n"
    "global_load_lds_dwordx4 v216, s[56:57] offset:2048 sc1\n"
    "v_exp_f32 v125, v125\n"
    "v_mfma_f32_16x16x32_bf16 v[104:107], a[236:239], v[160:163], v[104:107]\n"
    "v_exp_f32 v126, v126\n"
    "v_mfma_f32_16x16x32_bf16 v[108:111], a[240:243], v[160:163], v[108:111]\n"
    "v_exp_f32 v127, v127\n"
    "v_mfma_f32_16x16x32_bf16 v[128:131], a[48:51], a[12:15], 0\n"
    "ds_read_b128 a[96:99], v174 offset:24576\n"
    "v_cvt_pk_bf16_f32 v152, v112, v113\n"
    "v_mfma_f32_16x16x32_bf16 v[128:131], a[52:55], a[16:19], v[128:131]\n"
    "ds_read_b128 a[100:103], v174 offset:24640\n"
    "v_cvt_pk_bf16_f32 v153, v114, v115\n"
    "v_mfma_f32_16x16x32_bf16 v[128:131], a[56:59], a[20:23], v[128:131]\n"
    "ds_read_b128 a[104:107], v174 offset:24704\n"
    "v_cvt_pk_bf16_f32 v154, v116, v117\n"
    "v_mfma_f32_16x16x32_bf16 v[132:135], a[60:63], a[12:15], 0\n"
    "ds_read_b128 a[108:111], v174 offset:27648\n"
    "v_cvt_pk_bf16_f32 v155, v118, v119\n"
    "v_mfma_f32_16x16x32_bf16 v[132:135], a[64:67], a[16:19], v[132:135]\n"
    "ds_read_b128 a[112:115], v174 offset:27712\n"
    "v_cvt_pk_bf16_f32 v156, v120, v121\n"
    "v_mfma_f32_16x16x32_bf16 v[132:135], a[68:71], a[20:23], v[132:135]\n"
    "ds_read_b128 a[116:119], v174 offset:27776\n"
    "v_cvt_pk_bf16_f32 v157, v122, v123\n"
    "v_mfma_f32_16x16x32_bf16 v[136:139], a[72:75], a[12:15], 0\n"
    "ds_read_b128 a[120:123], v177 offset:24576\n"
    "v_cvt_pk_bf16_f32 v158, v124, v125\n"
    "v_mfma_f32_16x16x32_bf16 v[136:139], a[76:79], a[16:19], v[136:139]\n"
    "ds_read_b128 a[124:127], v177 offset:24640\n"
    "v_cvt_pk_bf16_f32 v159, v126, v127\n"
    "v_mfma_f32_16x16x32_bf16 v[136:139], a[80:83], a[20:23], v[136:139]\n"
    "v_exp_f32 v128, v128\n"
    "v_mfma_f32_16x16x32_bf16 v[140:143], a[84:87], a[12:15], 0\n"
    "v_exp_f32 v129, v129\n"
    "v_mfma_f32_16x16x32_bf16 v[140:143], a[88:91], a[16:19], v[140:143]\n"
    "v_exp_f32 v130, v130\n"
    "v_mfma_f32_16x16x32_bf16 v[140:143], a[92:95], a[20:23], v[140:143]\n"
    "v_exp_f32 v131, v131\n"
    "v_smfmac_f32_16x16x64_bf16 v[24:27], a[240:243], v[152:159], v239\n"
    "ds_read_b128 a[128:131], v177 offset:24704\n"
    "ds_read_b128 a[132:135], v177 offset:27648\n"
    "v_mfma_f32_16x16x32_bf16 v[0:3], a[144:147], v[152:155], v[0:3]\n"
    "v_exp_f32 v132, v132\n"
    "v_mfma_f32_16x16x32_bf16 v[0:3], a[168:171], v[156:159], v[0:3]\n"
    "v_exp_f32 v133, v133\n"
    "v_mfma_f32_16x16x32_bf16 v[4:7], a[148:151], v[152:155], v[4:7]\n"
    "v_exp_f32 v134, v134\n"
    "v_mfma_f32_16x16x32_bf16 v[4:7], a[172:175], v[156:159], v[4:7]\n"
    "ds_read_b128 a[136:139], v177 offset:27712\n"
    "v_exp_f32 v135, v135\n"
    "v_mfma_f32_16x16x32_bf16 v[8:11], a[152:155], v[152:155], v[8:11]\n"
    "s_waitcnt lgkmcnt(3)\n"
    "v_exp_f32 v136, v136\n"
    "v_mfma_f32_16x16x32_bf16 v[8:11], a[176:179], v[156:159], v[8:11]\n"
    "v_exp_f32 v137, v137\n"
    "v_mfma_f32_16x16x32_bf16 v[12:15], a[156:159], v[152:155], v[12:15]\n"
    "v_exp_f32 v138, v138\n"
    "v_mfma_f32_16x16x32_bf16 v[12:15], a[180:183], v[156:159], v[12:15]\n"
    "v_exp_f32 v139, v139\n"
    "v_mfma_f32_16x16x32_bf16 v[16:19], a[160:163], v[152:155], v[16:19]\n"
    "ds_read_b128 a[140:143], v177 offset:27776\n"
    "v_exp_f32 v140, v140\n"
    "v_mfma_f32_16x16x32_bf16 v[16:19], a[184:187], v[156:159], v[16:19]\n"
    "v_exp_f32 v141, v141\n"
    "v_mfma_f32_16x16x32_bf16 v[20:23], a[164:167], v[152:155], v[20:23]\n"
    "v_exp_f32 v142, v142\n"
    "v_mfma_f32_16x16x32_bf16 v[20:23], a[188:191], v[156:159], v[20:23]\n"
    "v_exp_f32 v143, v143\n"
    "v_mfma_f32_16x16x32_bf16 v[112:115], a[48:51], a[24:27], 0\n"
    "ds_read_b64_tr_b16 a[192:193], v175 offset:24576\n"
    "v_cvt_pk_bf16_f32 v152, v128, v129\n"
    "v_mfma_f32_16x16x32_bf16 v[112:115], a[52:55], a[28:31], v[112:115]\n"
    "ds_read_b64_tr_b16 a[194:195], v175 offset:27648\n"
    "v_cvt_pk_bf16_f32 v153, v130, v131\n"
    "v_mfma_f32_16x16x32_bf16 v[112:115], a[56:59], a[32:35], v[112:115]\n"
    "ds_read_b64_tr_b16 a[216:217], v178 offset:24576\n"
    "v_cvt_pk_bf16_f32 v154, v132, v133\n"
    "v_mfma_f32_16x16x32_bf16 v[116:119], a[60:63], a[24:27], 0\n"
    "ds_read_b64_tr_b16 a[218:219], v178 offset:27648\n"
    "v_cvt_pk_bf16_f32 v155, v134, v135\n"
    "v_mfma_f32_16x16x32_bf16 v[116:119], a[64:67], a[28:31], v[116:119]\n"
    "ds_read_b64_tr_b16 a[196:197], v176 offset:24576\n"
    "v_cvt_pk_bf16_f32 v156, v136, v137\n"
    "v_mfma_f32_16x16x32_bf16 v[116:119], a[68:71], a[32:35], v[116:119]\n"
    "ds_read_b64_tr_b16 a[198:199], v176 offset:27648\n"
    "v_cvt_pk_bf16_f32 v157, v138, v139\n"
    "v_mfma_f32_16x16x32_bf16 v[120:123], a[72:75], a[24:27], 0\n"
    "ds_read_b64_tr_b16 a[220:221], v179 offset:24576\n"
    "v_cvt_pk_bf16_f32 v158, v140, v141\n"
    "v_mfma_f32_16x16x32_bf16 v[120:123], a[76:79], a[28:31], v[120:123]\n"
    "ds_read_b64_tr_b16 a[222:223], v179 offset:27648\n"
    "v_cvt_pk_bf16_f32 v159, v142, v143\n"
    "v_mfma_f32_16x16x32_bf16 v[120:123], a[80:83], a[32:35], v[120:123]\n"
    "v_exp_f32 v112, v112\n"
    "v_mfma_f32_16x16x32_bf16 v[124:127], a[84:87], a[24:27], 0\n"
    "v_exp_f32 v113, v113\n"
    "v_mfma_f32_16x16x32_bf16 v[124:127], a[88:91], a[28:31], v[124:127]\n"
    "v_exp_f32 v114, v114\n"
    "v_mfma_f32_16x16x32_bf16 v[124:127], a[92:95], a[32:35], v[124:127]\n"
    "v_exp_f32 v115, v115\n"
    "v_smfmac_f32_16x16x64_bf16 v[52:55], a[240:243], v[152:159], v239\n"
    "ds_read_b64_tr_b16 a[200:201], v175 offset:24640\n"
    "ds_read_b64_tr_b16 a[202:203], v175 offset:27712\n"
    "v_mfma_f32_16x16x32_bf16 v[28:31], a[144:147], v[152:155], v[28:31]\n"
    "v_exp_f32 v116, v116\n"
    "v_mfma_f32_16x16x32_bf16 v[28:31], a[168:171], v[156:159], v[28:31]\n"
    "v_exp_f32 v117, v117\n"
    "v_mfma_f32_16x16x32_bf16 v[32:35], a[148:151], v[152:155], v[32:35]\n"
    "v_exp_f32 v118, v118\n"
    "v_mfma_f32_16x16x32_bf16 v[32:35], a[172:175], v[156:159], v[32:35]\n"
    "ds_read_b64_tr_b16 a[224:225], v178 offset:24640\n"
    "v_exp_f32 v119, v119\n"
    "v_mfma_f32_16x16x32_bf16 v[36:39], a[152:155], v[152:155], v[36:39]\n"
    "v_exp_f32 v120, v120\n"
    "v_mfma_f32_16x16x32_bf16 v[36:39], a[176:179], v[156:159], v[36:39]\n"
    "v_exp_f32 v121, v121\n"
    "v_mfma_f32_16x16x32_bf16 v[40:43], a[156:159], v[152:155], v[40:43]\n"
    "v_exp_f32 v122, v122\n"
    "v_mfma_f32_16x16x32_bf16 v[40:43], a[180:183], v[156:159], v[40:43]\n"
    "v_exp_f32 v123, v123\n"
    "v_mfma_f32_16x16x32_bf16 v[44:47], a[160:163], v[152:155], v[44:47]\n"
    "ds_read_b64_tr_b16 a[226:227], v178 offset:27712\n"
    "v_exp_f32 v124, v124\n"
    "v_mfma_f32_16x16x32_bf16 v[44:47], a[184:187], v[156:159], v[44:47]\n"
    "v_exp_f32 v125, v125\n"
    "v_mfma_f32_16x16x32_bf16 v[48:51], a[164:167], v[152:155], v[48:51]\n"
    "v_exp_f32 v126, v126\n"
    "v_mfma_f32_16x16x32_bf16 v[48:51], a[188:191], v[156:159], v[48:51]\n"
    "v_exp_f32 v127, v127\n"
    "v_mfma_f32_16x16x32_bf16 v[128:131], a[96:99], a[0:3], 0\n"
    "ds_read_b64_tr_b16 a[204:205], v176 offset:24640\n"
    "v_cvt_pk_bf16_f32 v152, v112, v113\n"
    "v_mfma_f32_16x16x32_bf16 v[128:131], a[100:103], a[4:7], v[128:131]\n"
    "ds_read_b64_tr_b16 a[206:207], v176 offset:27712\n"
    "v_cvt_pk_bf16_f32 v153, v114, v115\n"
    "v_mfma_f32_16x16x32_bf16 v[128:131], a[104:107], a[8:11], v[128:131]\n"
    "ds_read_b64_tr_b16 a[228:229], v179 offset:24640\n"
    "v_cvt_pk_bf16_f32 v154, v116, v117\n"
    "v_mfma_f32_16x16x32_bf16 v[132:135], a[108:111], a[0:3], 0\n"
    "ds_read_b64_tr_b16 a[230:231], v179 offset:27712\n"
    "v_cvt_pk_bf16_f32 v155, v118, v119\n"
    "v_mfma_f32_16x16x32_bf16 v[132:135], a[112:115], a[4:7], v[132:135]\n"
    "ds_read_b64_tr_b16 a[208:209], v175 offset:24704\n"
    "v_cvt_pk_bf16_f32 v156, v120, v121\n"
    "v_mfma_f32_16x16x32_bf16 v[132:135], a[116:119], a[8:11], v[132:135]\n"
    "ds_read_b64_tr_b16 a[210:211], v175 offset:27776\n"
    "v_cvt_pk_bf16_f32 v157, v122, v123\n"
    "v_mfma_f32_16x16x32_bf16 v[136:139], a[120:123], a[0:3], 0\n"
    "ds_read_b64_tr_b16 a[232:233], v178 offset:24704\n"
    "v_cvt_pk_bf16_f32 v158, v124, v125\n"
    "v_mfma_f32_16x16x32_bf16 v[136:139], a[124:127], a[4:7], v[136:139]\n"
    "s_waitcnt lgkmcnt(6)\n"
    "ds_read_b64_tr_b16 a[234:235], v178 offset:27776\n"
    "v_cvt_pk_bf16_f32 v159, v126, v127\n"
    "v_mfma_f32_16x16x32_bf16 v[136:139], a[128:131], a[8:11], v[136:139]\n"
    "v_exp_f32 v128, v128\n"
    "v_mfma_f32_16x16x32_bf16 v[140:143], a[132:135], a[0:3], 0\n"
    "v_exp_f32 v129, v129\n"
    "v_mfma_f32_16x16x32_bf16 v[140:143], a[136:139], a[4:7], v[140:143]\n"
    "v_exp_f32 v130, v130\n"
    "v_mfma_f32_16x16x32_bf16 v[140:143], a[140:143], a[8:11], v[140:143]\n"
    "v_exp_f32 v131, v131\n"
    "v_smfmac_f32_16x16x64_bf16 v[80:83], a[240:243], v[152:159], v239\n"
    "ds_read_b64_tr_b16 a[212:213], v176 offset:24704\n"
    "ds_read_b64_tr_b16 a[214:215], v176 offset:27776\n"
    "v_mfma_f32_16x16x32_bf16 v[56:59], a[144:147], v[152:155], v[56:59]\n"
    "v_exp_f32 v132, v132\n"
    "v_mfma_f32_16x16x32_bf16 v[56:59], a[168:171], v[156:159], v[56:59]\n"
    "v_exp_f32 v133, v133\n"
    "v_mfma_f32_16x16x32_bf16 v[60:63], a[148:151], v[152:155], v[60:63]\n"
    "v_exp_f32 v134, v134\n"
    "v_mfma_f32_16x16x32_bf16 v[60:63], a[172:175], v[156:159], v[60:63]\n"
    "ds_read_b64_tr_b16 a[236:237], v179 offset:24704\n"
    "v_exp_f32 v135, v135\n"
    "v_mfma_f32_16x16x32_bf16 v[64:67], a[152:155], v[152:155], v[64:67]\n"
    "v_exp_f32 v136, v136\n"
    "v_mfma_f32_16x16x32_bf16 v[64:67], a[176:179], v[156:159], v[64:67]\n"
    "v_exp_f32 v137, v137\n"
    "v_mfma_f32_16x16x32_bf16 v[68:71], a[156:159], v[152:155], v[68:71]\n"
    "v_exp_f32 v138, v138\n"
    "v_mfma_f32_16x16x32_bf16 v[68:71], a[180:183], v[156:159], v[68:71]\n"
    "v_exp_f32 v139, v139\n"
    "v_mfma_f32_16x16x32_bf16 v[72:75], a[160:163], v[152:155], v[72:75]\n"
    "ds_read_b64_tr_b16 a[238:239], v179 offset:27776\n"
    "v_exp_f32 v140, v140\n"
    "v_mfma_f32_16x16x32_bf16 v[72:75], a[184:187], v[156:159], v[72:75]\n"
    "v_exp_f32 v141, v141\n"
    "v_mfma_f32_16x16x32_bf16 v[76:79], a[164:167], v[152:155], v[76:79]\n"
    "v_exp_f32 v142, v142\n"
    "v_mfma_f32_16x16x32_bf16 v[76:79], a[188:191], v[156:159], v[76:79]\n"
    "v_exp_f32 v143, v143\n"
    "v_mfma_f32_16x16x32_bf16 v[112:115], a[96:99], a[12:15], 0\n"
    "s_waitcnt vmcnt(0)\n"
    "v_cvt_pk_bf16_f32 v152, v128, v129\n"
    "v_cvt_pk_bf16_f32 v153, v130, v131\n"
    "v_mfma_f32_16x16x32_bf16 v[112:115], a[100:103], a[16:19], v[112:115]\n"
    "s_barrier\n"
    "v_cvt_pk_bf16_f32 v154, v132, v133\n"
    "v_cvt_pk_bf16_f32 v155, v134, v135\n"
    "v_mfma_f32_16x16x32_bf16 v[112:115], a[104:107], a[20:23], v[112:115]\n"
    "ds_read_b128 a[48:51], v180 offset:0\n"
    "v_cvt_pk_bf16_f32 v156, v136, v137\n"
    "v_mfma_f32_16x16x32_bf16 v[116:119], a[108:111], a[12:15], 0\n"
    "ds_read_b128 a[52:55], v180 offset:64\n"
    "s_nop 0\n"
    "twins_p5:\n"
    "s_cbranch_execz twins_ret\n"
    "v_cvt_pk_bf16_f32 v157, v138, v139\n"
    "v_mfma_f32_16x16x32_bf16 v[116:119], a[112:115], a[16:19], v[116:119]\n"
    "ds_read_b128 a[56:59], v180 offset:128\n"
    "v_cvt_pk_bf16_f32 v158, v140, v141\n"
    "v_mfma_f32_16x16x32_bf16 v[116:119], a[116:119], a[20:23], v[116:119]\n"
    "ds_read_b128 a[60:63], v180 offset:3072\n"
    "v_cvt_pk_bf16_f32 v159, v142, v143\n"
    "v_mfma_f32_16x16x32_bf16 v[120:123], a[120:123], a[12:15], 0\n"
    "v_exp_f32 v112, v112\n"
    "v_mfma_f32_16x16x32_bf16 v[120:123], a[124:127], a[16:19], v[120:123]\n"
    "v_exp_f32 v113, v113\n"
    "v_mfma_f32_16x16x32_bf16 v[120:123], a[128:131], a[20:23], v[120:123]\n"
    "ds_read_b128 a[64:67], v180 offset:3136\n"
    "ds_read_b128 a[68:71], v180 offset:3200\n"
    "v_mfma_f32_16x16x32_bf16 v[124:127], a[132:135], a[12:15], 0\n"
    "v_exp_f32 v114, v114\n"
    "v_mfma_f32_16x16x32_bf16 v[124:127], a[136:139], a[16:19], v[124:127]\n"
    "v_exp_f32 v115, v115\n"
    "v_mfma_f32_16x16x32_bf16 v[124:127], a[140:143], a[20:23], v[124:127]\n"
    "v_exp_f32 v116, v116\n"
    "v_smfmac_f32_16x16x64_bf16 v[24:27], a[240:243], v[152:159], v239\n"
    "ds_read_b128 a[72:75], v183 offset:0\n"
    "ds_read_b128 a[76:79], v183 offset:64\n"
    "v_mfma_f32_16x16x32_bf16 v[0:3], a[192:195], v[152:155], v[0:3]\n"
    "v_exp_f32 v117, v117\n"
    "v_mfma_f32_16x16x32_bf16 v[0:3], a[216:219], v[156:159], v[0:3]\n"
    "v_exp_f32 v118, v118\n"
    "v_mfma_f32_16x16x32_bf16 v[4:7], a[196:199], v[152:155], v[4:7]\n"
    "v_exp_f32 v119, v119\n"
    "v_mfma_f32_16x16x32_bf16 v[4:7], a[220:223], v[156:159], v[4:7]\n"
    "v_exp_f32 v120, v120\n"
    "v_mfma_f32_16x16x32_bf16 v[8:11], a[200:203], v[152:155], v[8:11]\n"
    "ds_read_b128 a[80:83], v183 offset:128\n"
    "ds_read_b128 a[84:87], v183 offset:3072\n"
    "v_mfma_f32_16x16x32_bf16 v[8:11], a[224:227], v[156:159], v[8:11]\n"
    "s_waitcnt lgkmcnt(4)\n"
    "v_exp_f32 v121, v121\n"
    "v_mfma_f32_16x16x32_bf16 v[12:15], a[204:207], v[152:155], v[12:15]\n"
    "v_exp_f32 v122, v122\n"
    "v_mfma_f32_16x16x32_bf16 v[12:15], a[228:231], v[156:159], v[12:15]\n"
    "ds_read_b128 a[88:91], v183 offset:3136\n"
    "v_exp_f32 v123, v123\n"
    "v_mfma_f32_16x16x32_bf16 v[16:19], a[208:211], v[152:155], v[16:19]\n"
    "v_exp_f32 v124, v124\n"
    "v_mfma_f32_16x16x32_bf16 v[16:19], a[232:235], v[156:159], v[16:19]\n"
    "v_exp_f32 v125, v125\n"
    "v_mfma_f32_16x16x32_bf16 v[20:23], a[212:215], v[152:155], v[20:23]\n"
    "ds_read_b128 a[92:95], v183 offset:3200\n"
    "v_exp_f32 v126, v126\n"
    "v_mfma_f32_16x16x32_bf16 v[20:23], a[236:239], v[156:159], v[20:23]\n"
    "v_exp_f32 v127, v127\n"
    "v_mfma_f32_16x16x32_bf16 v[128:131], a[96:99], a[24:27], 0\n"
    "ds_read_b64_tr_b16 a[144:145], v181 offset:0\n"
    "v_cvt_pk_bf16_f32 v152, v112, v113\n"
    "v_mfma_f32_16x16x32_bf16 v[128:131], a[100:103], a[28:31], v[128:131]\n"
    "ds_read_b64_tr_b16 a[146:147], v181 offset:3072\n"
    "v_cvt_pk_bf16_f32 v153, v114, v115\n"
    "v_mfma_f32_16x16x32_bf16 v[128:131], a[104:107], a[32:35], v[128:131]\n"
    "ds_read_b64_tr_b16 a[168:169], v184 offset:0\n"
    "v_cvt_pk_bf16_f32 v154, v116, v117\n"
    "v_mfma_f32_16x16x32_bf16 v[132:135], a[108:111], a[24:27], 0\n"
    "ds_read_b64_tr_b16 a[170:171], v184 offset:3072\n"
    "v_cvt_pk_bf16_f32 v155, v118, v119\n"
    "v_mfma_f32_16x16x32_bf16 v[132:135], a[112:115], a[28:31], v[132:135]\n"
    "ds_read_b64_tr_b16 a[148:149], v182 offset:0\n"
    "v_cvt_pk_bf16_f32 v156, v120, v121\n"
    "v_mfma_f32_16x16x32_bf16 v[132:135], a[116:119], a[32:35], v[132:135]\n"
    "ds_read_b64_tr_b16 a[150:151], v182 offset:3072\n"
    "v_cvt_pk_bf16_f32 v157, v122, v123\n"
    "v_mfma_f32_16x16x32_bf16 v[136:139], a[120:123], a[24:27], 0\n"
    "ds_read_b64_tr_b16 a[172:173], v185 offset:0\n"
    "v_cvt_pk_bf16_f32 v158, v124, v125\n"
    "v_mfma_f32_16x16x32_bf16 v[136:139], a[124:127], a[28:31], v[136:139]\n"
    "ds_read_b64_tr_b16 a[174:175], v185 offset:3072\n"
    "v_cvt_pk_bf16_f32 v159, v126, v127\n"
    "v_mfma_f32_16x16x32_bf16 v[136:139], a[128:131], a[32:35], v[136:139]\n"
    "v_exp_f32 v128, v128\n"
    "v_mfma_f32_16x16x32_bf16 v[140:143], a[132:135], a[24:27], 0\n"
    "v_exp_f32 v129, v129\n"
    "v_mfma_f32_16x16x32_bf16 v[140:143], a[136:139], a[28:31], v[140:143]\n"
    "v_exp_f32 v130, v130\n"
    "v_mfma_f32_16x16x32_bf16 v[140:143], a[140:143], a[32:35], v[140:143]\n"
    "v_exp_f32 v131, v131\n"
    "v_mfma_f32_16x16x32_bf16 v[144:147], a[120:123], a[36:39], v[164:167]\n"
    "v_exp_f32 v132, v132\n"
    "v_mfma_f32_16x16x32_bf16 v[144:147], a[124:127], a[40:43], v[144:147]\n"
    "v_exp_f32 v133, v133\n"
    "v_mfma_f32_16x16x32_bf16 v[144:147], a[128:131], a[44:47], v[144:147]\n"
    "v_exp_f32 v134, v134\n"
    "v_mfma_f32_16x16x32_bf16 v[148:151], a[132:135], a[36:39], v[164:167]\n"
    "v_exp_f32 v135, v135\n"
    "v_mfma_f32_16x16x32_bf16 v[148:151], a[136:139], a[40:43], v[148:151]\n"
    "v_exp_f32 v136, v136\n"
    "v_mfma_f32_16x16x32_bf16 v[148:151], a[140:143], a[44:47], v[148:151]\n"
    "v_exp_f32 v137, v137\n"
    "v_smfmac_f32_16x16x64_bf16 v[52:55], a[240:243], v[152:159], v239\n"
    "ds_read_b64_tr_b16 a[152:153], v181 offset:64\n"
    "ds_read_b64_tr_b16 a[154:155], v181 offset:3136\n"
    "v_mfma_f32_16x16x32_bf16 v[28:31], a[192:195], v[152:155], v[28:31]\n"
    "v_exp_f32 v138, v138\n"
    "v_mfma_f32_16x16x32_bf16 v[28:31], a[216:219], v[156:159], v[28:31]\n"
    "v_exp_f32 v139, v139\n"
    "v_mfma_f32_16x16x32_bf16 v[32:35], a[196:199], v[152:155], v[32:35]\n"
    "v_exp_f32 v140, v140\n"
    "v_mfma_f32_16x16x32_bf16 v[32:35], a[220:223], v[156:159], v[32:35]\n"
    "v_exp_f32 v141, v141\n"
    "v_mfma_f32_16x16x32_bf16 v[36:39], a[200:203], v[152:155], v[36:39]\n"
    "v_exp_f32 v142, v142\n"
    "v_mfma_f32_16x16x32_bf16 v[36:39], a[224:227], v[156:159], v[36:39]\n"
    "v_exp_f32 v143, v143\n"
    "v_mfma_f32_16x16x32_bf16 v[40:43], a[204:207], v[152:155], v[40:43]\n"
    "v_exp_f32 v144, v144\n"
    "v_mfma_f32_16x16x32_bf16 v[40:43], a[228:231], v[156:159], v[40:43]\n"
    "v_exp_f32 v145, v145\n"
    "v_mfma_f32_16x16x32_bf16 v[44:47], a[208:211], v[152:155], v[44:47]\n"
    "v_exp_f32 v146, v146\n"
    "v_mfma_f32_16x16x32_bf16 v[44:47], a[232:235], v[156:159], v[44:47]\n"
    "v_exp_f32 v147, v147\n"
    "v_mfma_f32_16x16x32_bf16 v[48:51], a[212:215], v[152:155], v[48:51]\n"
    "v_exp_f32 v148, v148\n"
    "v_mfma_f32_16x16x32_bf16 v[48:51], a[236:239], v[156:159], v[48:51]\n"
    "v_exp_f32 v149, v149\n"
    "v_mfma_f32_16x16x32_bf16 v[112:115], a[48:51], a[0:3], 0\n"
    "ds_read_b64_tr_b16 a[176:177], v184 offset:64\n"
    "v_cvt_pk_bf16_f32 v152, v128, v129\n"
    "v_mfma_f32_16x16x32_bf16 v[112:115], a[52:55], a[4:7], v[112:115]\n"
    "ds_read_b64_tr_b16 a[178:179], v184 offset:3136\n"
    "v_cvt_pk_bf16_f32 v153, v130, v131\n"
    "v_mfma_f32_16x16x32_bf16 v[112:115], a[56:59], a[8:11], v[112:115]\n"
    "ds_read_b64_tr_b16 a[156:157], v182 offset:64\n"
    "v_cvt_pk_bf16_f32 v154, v132, v133\n"
    "v_mfma_f32_16x16x32_bf16 v[116:119], a[60:63], a[0:3], 0\n"
    "ds_read_b64_tr_b16 a[158:159], v182 offset:3136\n"
    "v_cvt_pk_bf16_f32 v155, v134, v135\n"
    "v_mfma_f32_16x16x32_bf16 v[116:119], a[64:67], a[4:7], v[116:119]\n"
    "ds_read_b64_tr_b16 a[180:181], v185 offset:64\n"
    "v_cvt_pk_bf16_f32 v156, v136, v137\n"
    "v_mfma_f32_16x16x32_bf16 v[116:119], a[68:71], a[8:11], v[116:119]\n"
    "s_waitcnt lgkmcnt(5)\n"
    "ds_read_b64_tr_b16 a[182:183], v185 offset:3136\n"
    "v_cvt_pk_bf16_f32 v157, v138, v139\n"
    "v_mfma_f32_16x16x32_bf16 v[120:123], a[72:75], a[0:3], 0\n"
    "ds_read_b64_tr_b16 a[160:161], v181 offset:128\n"
    "v_cvt_pk_bf16_f32 v158, v140, v141\n"
    "v_mfma_f32_16x16x32_bf16 v[120:123], a[76:79], a[4:7], v[120:123]\n"
    "ds_read_b64_tr_b16 a[162:163], v181 offset:3200\n"
    "v_cvt_pk_bf16_f32 v159, v142, v143\n"
    "v_mfma_f32_16x16x32_bf16 v[120:123], a[80:83], a[8:11], v[120:123]\n"
    "v_exp_f32 v150, v150\n"
    "v_mfma_f32_16x16x32_bf16 v[124:127], a[84:87], a[0:3], 0\n"
    "v_exp_f32 v151, v151\n"
    "v_mfma_f32_16x16x32_bf16 v[124:127], a[88:91], a[4:7], v[124:127]\n"
    "ds_read_b64_tr_b16 a[184:185], v184 offset:128\n"
    "v_cvt_pk_bf16_f32 v160, v144, v145\n"
    "v_mfma_f32_16x16x32_bf16 v[124:127], a[92:95], a[8:11], v[124:127]\n"
    "ds_read_b64_tr_b16 a[186:187], v184 offset:3200\n"
    "v_cvt_pk_bf16_f32 v161, v146, v147\n"
    "v_smfmac_f32_16x16x64_bf16 v[80:83], a[240:243], v[152:159], v239\n"
    "ds_read_b64_tr_b16 a[164:165], v182 offset:128\n"
    "v_cvt_pk_bf16_f32 v162, v148, v149\n"
    "v_mfma_f32_16x16x32_bf16 v[56:59], a[192:195], v[152:155], v[56:59]\n"
    "ds_read_b64_tr_b16 a[166:167], v182 offset:3200\n"
    "v_cvt_pk_bf16_f32 v163, v150, v151\n"
    "v_mfma_f32_16x16x32_bf16 v[56:59], a[216:219], v[156:159], v[56:59]\n"
    "v_exp_f32 v112, v112\n"
    "v_mfma_f32_16x16x32_bf16 v[60:63], a[196:199], v[152:155], v[60:63]\n"
    "v_exp_f32 v113, v113\n"
    "v_mfma_f32_16x16x32_bf16 v[60:63], a[220:223], v[156:159], v[60:63]\n"
    "v_exp_f32 v114, v114\n"
    "v_mfma_f32_16x16x32_bf16 v[64:67], a[200:203], v[152:155], v[64:67]\n"
    "ds_read_b64_tr_b16 a[188:189], v185 offset:128\n"
    "ds_read_b64_tr_b16 a[190:191], v185 offset:3200\n"
    "v_mfma_f32_16x16x32_bf16 v[64:67], a[224:227], v[156:159], v[64:67]\n"
    "v_exp_f32 v115, v115\n"
    "v_mfma_f32_16x16x32_bf16 v[68:71], a[204:207], v[152:155], v[68:71]\n"
    "v_exp_f32 v116, v116\n"
    "v_mfma_f32_16x16x32_bf16 v[68:71], a[228:231], v[156:159], v[68:71]\n"
    "v_exp_f32 v117, v117\n"
    "v_mfma_f32_16x16x32_bf16 v[72:75], a[208:211], v[152:155], v[72:75]\n"
    "v_exp_f32 v118, v118\n"
    "v_mfma_f32_16x16x32_bf16 v[72:75], a[232:235], v[156:159], v[72:75]\n"
    "v_exp_f32 v119, v119\n"
    "v_mfma_f32_16x16x32_bf16 v[76:79], a[212:215], v[152:155], v[76:79]\n"
    "v_exp_f32 v120, v120\n"
    "v_mfma_f32_16x16x32_bf16 v[76:79], a[236:239], v[156:159], v[76:79]\n"
    "v_mfma_f32_16x16x32_bf16 v[84:87], a[216:219], v[160:163], v[84:87]\n"
    "v_exp_f32 v121, v121\n"
    "v_mfma_f32_16x16x32_bf16 v[88:91], a[220:223], v[160:163], v[88:91]\n"
    "v_exp_f32 v122, v122\n"
    "v_mfma_f32_16x16x32_bf16 v[92:95], a[224:227], v[160:163], v[92:95]\n"
    "v_exp_f32 v123, v123\n"
    "v_mfma_f32_16x16x32_bf16 v[96:99], a[228:231], v[160:163], v[96:99]\n"
    "v_exp_f32 v124, v124\n"
    "v_mfma_f32_16x16x32_bf16 v[100:103], a[232:235], v[160:163], v[100:103]\n"
    "v_exp_f32 v125, v125\n"
    "v_mfma_f32_16x16x32_bf16 v[104:107], a[236:239], v[160:163], v[104:107]\n"
    "v_exp_f32 v126, v126\n"
    "v_mfma_f32_16x16x32_bf16 v[108:111], a[240:243], v[160:163], v[108:111]\n"
    "v_exp_f32 v127, v127\n"
    "v_mfma_f32_16x16x32_bf16 v[128:131], a[48:51], a[12:15], 0\n"
    "ds_read_b128 a[96:99], v186 offset:0\n"
    "v_cvt_pk_bf16_f32 v152, v112, v113\n"
    "v_mfma_f32_16x16x32_bf16 v[128:131], a[52:55], a[16:19], v[128:131]\n"
    "ds_read_b128 a[100:103], v186 offset:64\n"
    "v_cvt_pk_bf16_f32 v153, v114, v115\n"
    "v_mfma_f32_16x16x32_bf16 v[128:131], a[56:59], a[20:23], v[128:131]\n"
    "ds_read_b128 a[104:107], v186 offset:128\n"
    "v_cvt_pk_bf16_f32 v154, v116, v117\n"
    "v_mfma_f32_16x16x32_bf16 v[132:135], a[60:63], a[12:15], 0\n"
    "ds_read_b128 a[108:111], v186 offset:3072\n"
    "v_cvt_pk_bf16_f32 v155, v118, v119\n"
    "v_mfma_f32_16x16x32_bf16 v[132:135], a[64:67], a[16:19], v[132:135]\n"
    "ds_read_b128 a[112:115], v186 offset:3136\n"
    "v_cvt_pk_bf16_f32 v156, v120, v121\n"
    "v_mfma_f32_16x16x32_bf16 v[132:135], a[68:71], a[20:23], v[132:135]\n"
    "ds_read_b128 a[116:119], v186 offset:3200\n"
    "v_cvt_pk_bf16_f32 v157, v122, v123\n"
    "v_mfma_f32_16x16x32_bf16 v[136:139], a[72:75], a[12:15], 0\n"
    "ds_read_b128 a[120:123], v189 offset:0\n"
    "v_cvt_pk_bf16_f32 v158, v124, v125\n"
    "v_mfma_f32_16x16x32_bf16 v[136:139], a[76:79], a[16:19], v[136:139]\n"
    "ds_read_b128 a[124:127], v189 offset:64\n"
    "v_cvt_pk_bf16_f32 v159, v126, v127\n"
    "v_mfma_f32_16x16x32_bf16 v[136:139], a[80:83], a[20:23], v[136:139]\n"
    "v_exp_f32 v128, v128\n"
    "v_mfma_f32_16x16x32_bf16 v[140:143], a[84:87], a[12:15], 0\n"
    "v_exp_f32 v129, v129\n"
    "v_mfma_f32_16x16x32_bf16 v[140:143], a[88:91], a[16:19], v[140:143]\n"
    "v_exp_f32 v130, v130\n"
    "v_mfma_f32_16x16x32_bf16 v[140:143], a[92:95], a[20:23], v[140:143]\n"
    "v_exp_f32 v131, v131\n"
    "v_smfmac_f32_16x16x64_bf16 v[24:27], a[240:243], v[152:159], v239\n"
    "ds_read_b128 a[128:131], v189 offset:128\n"
    "ds_read_b128 a[132:135], v189 offset:3072\n"
    "v_mfma_f32_16x16x32_bf16 v[0:3], a[144:147], v[152:155], v[0:3]\n"
    "v_exp_f32 v132, v132\n"
    "v_mfma_f32_16x16x32_bf16 v[0:3], a[168:171], v[156:159], v[0:3]\n"
    "v_exp_f32 v133, v133\n"
    "v_mfma_f32_16x16x32_bf16 v[4:7], a[148:151], v[152:155], v[4:7]\n"
    "v_exp_f32 v134, v134\n"
    "v_mfma_f32_16x16x32_bf16 v[4:7], a[172:175], v[156:159], v[4:7]\n"
    "ds_read_b128 a[136:139], v189 offset:3136\n"
    "v_exp_f32 v135, v135\n"
    "v_mfma_f32_16x16x32_bf16 v[8:11], a[152:155], v[152:155], v[8:11]\n"
    "s_waitcnt lgkmcnt(3)\n"
    "v_exp_f32 v136, v136\n"
    "v_mfma_f32_16x16x32_bf16 v[8:11], a[176:179], v[156:159], v[8:11]\n"
    "v_exp_f32 v137, v137\n"
    "v_mfma_f32_16x16x32_bf16 v[12:15], a[156:159], v[152:155], v[12:15]\n"
    "v_exp_f32 v138, v138\n"
    "v_mfma_f32_16x16x32_bf16 v[12:15], a[180:183], v[156:159], v[12:15]\n"
    "v_exp_f32 v139, v139\n"
    "v_mfma_f32_16x16x32_bf16 v[16:19], a[160:163], v[152:155], v[16:19]\n"
    "ds_read_b128 a[140:143], v189 offset:3200\n"
    "v_exp_f32 v140, v140\n"
    "v_mfma_f32_16x16x32_bf16 v[16:19], a[184:187], v[156:159], v[16:19]\n"
    "v_exp_f32 v141, v141\n"
    "v_mfma_f32_16x16x32_bf16 v[20:23], a[164:167], v[152:155], v[20:23]\n"
    "v_exp_f32 v142, v142\n"
    "v_mfma_f32_16x16x32_bf16 v[20:23], a[188:191], v[156:159], v[20:23]\n"
    "v_exp_f32 v143, v143\n"
    "v_mfma_f32_16x16x32_bf16 v[112:115], a[48:51], a[24:27], 0\n"
    "ds_read_b64_tr_b16 a[192:193], v187 offset:0\n"
    "v_cvt_pk_bf16_f32 v152, v128, v129\n"
    "v_mfma_f32_16x16x32_bf16 v[112:115], a[52:55], a[28:31], v[112:115]\n"
    "ds_read_b64_tr_b16 a[194:195], v187 offset:3072\n"
    "v_cvt_pk_bf16_f32 v153, v130, v131\n"
    "v_mfma_f32_16x16x32_bf16 v[112:115], a[56:59], a[32:35], v[112:115]\n"
    "ds_read_b64_tr_b16 a[216:217], v190 offset:0\n"
    "v_cvt_pk_bf16_f32 v154, v132, v133\n"
    "v_mfma_f32_16x16x32_bf16 v[116:119], a[60:63], a[24:27], 0\n"
    "ds_read_b64_tr_b16 a[218:219], v190 offset:3072\n"
    "v_cvt_pk_bf16_f32 v155, v134, v135\n"
    "v_mfma_f32_16x16x32_bf16 v[116:119], a[64:67], a[28:31], v[116:119]\n"
    "ds_read_b64_tr_b16 a[196:197], v188 offset:0\n"
    "v_cvt_pk_bf16_f32 v156, v136, v137\n"
    "v_mfma_f32_16x16x32_bf16 v[116:119], a[68:71], a[32:35], v[116:119]\n"
    "ds_read_b64_tr_b16 a[198:199], v188 offset:3072\n"
    "v_cvt_pk_bf16_f32 v157, v138, v139\n"
    "v_mfma_f32_16x16x32_bf16 v[120:123], a[72:75], a[24:27], 0\n"
    "ds_read_b64_tr_b16 a[220:221], v191 offset:0\n"
    "v_cvt_pk_bf16_f32 v158, v140, v141\n"
    "v_mfma_f32_16x16x32_bf16 v[120:123], a[76:79], a[28:31], v[120:123]\n"
    "ds_read_b64_tr_b16 a[222:223], v191 offset:3072\n"
    "v_cvt_pk_bf16_f32 v159, v142, v143\n"
    "v_mfma_f32_16x16x32_bf16 v[120:123], a[80:83], a[32:35], v[120:123]\n"
    "v_exp_f32 v112, v112\n"
    "v_mfma_f32_16x16x32_bf16 v[124:127], a[84:87], a[24:27], 0\n"
    "v_exp_f32 v113, v113\n"
    "v_mfma_f32_16x16x32_bf16 v[124:127], a[88:91], a[28:31], v[124:127]\n"
    "v_exp_f32 v114, v114\n"
    "v_mfma_f32_16x16x32_bf16 v[124:127], a[92:95], a[32:35], v[124:127]\n"
    "v_exp_f32 v115, v115\n"
    "v_smfmac_f32_16x16x64_bf16 v[52:55], a[240:243], v[152:159], v239\n"
    "ds_read_b64_tr_b16 a[200:201], v187 offset:64\n"
    "ds_read_b64_tr_b16 a[202:203], v187 offset:3136\n"
    "v_mfma_f32_16x16x32_bf16 v[28:31], a[144:147], v[152:155], v[28:31]\n"
    "v_exp_f32 v116, v116\n"
    "v_mfma_f32_16x16x32_bf16 v[28:31], a[168:171], v[156:159], v[28:31]\n"
    "v_exp_f32 v117, v117\n"
    "v_mfma_f32_16x16x32_bf16 v[32:35], a[148:151], v[152:155], v[32:35]\n"
    "v_exp_f32 v118, v118\n"
    "v_mfma_f32_16x16x32_bf16 v[32:35], a[172:175], v[156:159], v[32:35]\n"
    "ds_read_b64_tr_b16 a[224:225], v190 offset:64\n"
    "v_exp_f32 v119, v119\n"
    "v_mfma_f32_16x16x32_bf16 v[36:39], a[152:155], v[152:155], v[36:39]\n"
    "v_exp_f32 v120, v120\n"
    "v_mfma_f32_16x16x32_bf16 v[36:39], a[176:179], v[156:159], v[36:39]\n"
    "v_exp_f32 v121, v121\n"
    "v_mfma_f32_16x16x32_bf16 v[40:43], a[156:159], v[152:155], v[40:43]\n"
    "v_exp_f32 v122, v122\n"
    "v_mfma_f32_16x16x32_bf16 v[40:43], a[180:183], v[156:159], v[40:43]\n"
    "v_exp_f32 v123, v123\n"
    "v_mfma_f32_16x16x32_bf16 v[44:47], a[160:163], v[152:155], v[44:47]\n"
    "ds_read_b64_tr_b16 a[226:227], v190 offset:3136\n"
    "v_exp_f32 v124, v124\n"
    "v_mfma_f32_16x16x32_bf16 v[44:47], a[184:187], v[156:159], v[44:47]\n"
    "v_exp_f32 v125, v125\n"
    "v_mfma_f32_16x16x32_bf16 v[48:51], a[164:167], v[152:155], v[48:51]\n"
    "v_exp_f32 v126, v126\n"
    "v_mfma_f32_16x16x32_bf16 v[48:51], a[188:191], v[156:159], v[48:51]\n"
    "v_exp_f32 v127, v127\n"
    "v_mfma_f32_16x16x32_bf16 v[128:131], a[96:99], a[0:3], 0\n"
    "ds_read_b64_tr_b16 a[204:205], v188 offset:64\n"
    "v_cvt_pk_bf16_f32 v152, v112, v113\n"
    "v_mfma_f32_16x16x32_bf16 v[128:131], a[100:103], a[4:7], v[128:131]\n"
    "ds_read_b64_tr_b16 a[206:207], v188 offset:3136\n"
    "v_cvt_pk_bf16_f32 v153, v114, v115\n"
    "v_mfma_f32_16x16x32_bf16 v[128:131], a[104:107], a[8:11], v[128:131]\n"
    "ds_read_b64_tr_b16 a[228:229], v191 offset:64\n"
    "v_cvt_pk_bf16_f32 v154, v116, v117\n"
    "v_mfma_f32_16x16x32_bf16 v[132:135], a[108:111], a[0:3], 0\n"
    "ds_read_b64_tr_b16 a[230:231], v191 offset:3136\n"
    "v_cvt_pk_bf16_f32 v155, v118, v119\n"
    "v_mfma_f32_16x16x32_bf16 v[132:135], a[112:115], a[4:7], v[132:135]\n"
    "ds_read_b64_tr_b16 a[208:209], v187 offset:128\n"
    "v_cvt_pk_bf16_f32 v156, v120, v121\n"
    "v_mfma_f32_16x16x32_bf16 v[132:135], a[116:119], a[8:11], v[132:135]\n"
    "ds_read_b64_tr_b16 a[210:211], v187 offset:3200\n"
    "v_cvt_pk_bf16_f32 v157, v122, v123\n"
    "v_mfma_f32_16x16x32_bf16 v[136:139], a[120:123], a[0:3], 0\n"
    "ds_read_b64_tr_b16 a[232:233], v190 offset:128\n"
    "v_cvt_pk_bf16_f32 v158, v124, v125\n"
    "v_mfma_f32_16x16x32_bf16 v[136:139], a[124:127], a[4:7], v[136:139]\n"
    "s_waitcnt lgkmcnt(6)\n"
    "ds_read_b64_tr_b16 a[234:235], v190 offset:3200\n"
    "v_cvt_pk_bf16_f32 v159, v126, v127\n"
    "v_mfma_f32_16x16x32_bf16 v[136:139], a[128:131], a[8:11], v[136:139]\n"
    "v_exp_f32 v128, v128\n"
    "v_mfma_f32_16x16x32_bf16 v[140:143], a[132:135], a[0:3], 0\n"
    "v_exp_f32 v129, v129\n"
    "v_mfma_f32_16x16x32_bf16 v[140:143], a[136:139], a[4:7], v[140:143]\n"
    "v_exp_f32 v130, v130\n"
    "v_mfma_f32_16x16x32_bf16 v[140:143], a[140:143], a[8:11], v[140:143]\n"
    "v_exp_f32 v131, v131\n"
    "v_smfmac_f32_16x16x64_bf16 v[80:83], a[240:243], v[152:159], v239\n"
    "ds_read_b64_tr_b16 a[212:213], v188 offset:128\n"
    "ds_read_b64_tr_b16 a[214:215], v188 offset:3200\n"
    "v_mfma_f32_16x16x32_bf16 v[56:59], a[144:147], v[152:155], v[56:59]\n"
    "v_exp_f32 v132, v132\n"
    "v_mfma_f32_16x16x32_bf16 v[56:59], a[168:171], v[156:159], v[56:59]\n"
    "v_exp_f32 v133, v133\n"
    "v_mfma_f32_16x16x32_bf16 v[60:63], a[148:151], v[152:155], v[60:63]\n"
    "v_exp_f32 v134, v134\n"
    "v_mfma_f32_16x16x32_bf16 v[60:63], a[172:175], v[156:159], v[60:63]\n"
    "ds_read_b64_tr_b16 a[236:237], v191 offset:128\n"
    "v_exp_f32 v135, v135\n"
    "v_mfma_f32_16x16x32_bf16 v[64:67], a[152:155], v[152:155], v[64:67]\n"
    "v_exp_f32 v136, v136\n"
    "v_mfma_f32_16x16x32_bf16 v[64:67], a[176:179], v[156:159], v[64:67]\n"
    "v_exp_f32 v137, v137\n"
    "v_mfma_f32_16x16x32_bf16 v[68:71], a[156:159], v[152:155], v[68:71]\n"
    "v_exp_f32 v138, v138\n"
    "v_mfma_f32_16x16x32_bf16 v[68:71], a[180:183], v[156:159], v[68:71]\n"
    "v_exp_f32 v139, v139\n"
    "v_mfma_f32_16x16x32_bf16 v[72:75], a[160:163], v[152:155], v[72:75]\n"
    "ds_read_b64_tr_b16 a[238:239], v191 offset:3200\n"
    "v_exp_f32 v140, v140\n"
    "v_mfma_f32_16x16x32_bf16 v[72:75], a[184:187], v[156:159], v[72:75]\n"
    "v_exp_f32 v141, v141\n"
    "v_mfma_f32_16x16x32_bf16 v[76:79], a[164:167], v[152:155], v[76:79]\n"
    "v_exp_f32 v142, v142\n"
    "v_mfma_f32_16x16x32_bf16 v[76:79], a[188:191], v[156:159], v[76:79]\n"
    "v_exp_f32 v143, v143\n"
    "v_mfma_f32_16x16x32_bf16 v[112:115], a[96:99], a[12:15], 0\n"
    "ds_read_b128 a[48:51], v180 offset:24576\n"
    "v_cvt_pk_bf16_f32 v152, v128, v129\n"
    "v_mfma_f32_16x16x32_bf16 v[112:115], a[100:103], a[16:19], v[112:115]\n"
    "ds_read_b128 a[52:55], v180 offset:24640\n"
    "v_cvt_pk_bf16_f32 v153, v130, v131\n"
    "v_mfma_f32_16x16x32_bf16 v[112:115], a[104:107], a[20:23], v[112:115]\n"
    "ds_read_b128 a[56:59], v180 offset:24704\n"
    "v_cvt_pk_bf16_f32 v154, v132, v133\n"
    "v_mfma_f32_16x16x32_bf16 v[116:119], a[108:111], a[12:15], 0\n"
    "ds_read_b128 a[60:63], v180 offset:27648\n"
    "v_cvt_pk_bf16_f32 v155, v134, v135\n"
    "v_mfma_f32_16x16x32_bf16 v[116:119], a[112:115], a[16:19], v[116:119]\n"
    "ds_read_b128 a[64:67], v180 offset:27712\n"
    "v_cvt_pk_bf16_f32 v156, v136, v137\n"
    "v_mfma_f32_16x16x32_bf16 v[116:119], a[116:119], a[20:23], v[116:119]\n"
    "ds_read_b128 a[68:71], v180 offset:27776\n"
    "v_cvt_pk_bf16_f32 v157, v138, v139\n"
    "v_mfma_f32_16x16x32_bf16 v[120:123], a[120:123], a[12:15], 0\n"
    "ds_read_b128 a[72:75], v183 offset:24576\n"
    "v_cvt_pk_bf16_f32 v158, v140, v141\n"
    "v_mfma_f32_16x16x32_bf16 v[120:123], a[124:127], a[16:19], v[120:123]\n"
    "ds_read_b128 a[76:79], v183 offset:24640\n"
    "v_cvt_pk_bf16_f32 v159, v142, v143\n"
    "v_mfma_f32_16x16x32_bf16 v[120:123], a[128:131], a[20:23], v[120:123]\n"
    "v_exp_f32 v112, v112\n"
    "v_mfma_f32_16x16x32_bf16 v[124:127], a[132:135], a[12:15], 0\n"
    "v_exp_f32 v113, v113\n"
    "v_mfma_f32_16x16x32_bf16 v[124:127], a[136:139], a[16:19], v[124:127]\n"
    "v_exp_f32 v114, v114\n"
    "v_mfma_f32_16x16x32_bf16 v[124:127], a[140:143], a[20:23], v[124:127]\n"
    "v_exp_f32 v115, v115\n"
    "v_smfmac_f32_16x16x64_bf16 v[24:27], a[240:243], v[152:159], v239\n"
    "ds_read_b128 a[80:83], v183 offset:24704\n"
    "ds_read_b128 a[84:87], v183 offset:27648\n"
    "v_mfma_f32_16x16x32_bf16 v[0:3], a[192:195], v[152:155], v[0:3]\n"
    "v_exp_f32 v116, v116\n"
    "v_mfma_f32_16x16x32_bf16 v[0:3], a[216:219], v[156:159], v[0:3]\n"
    "v_exp_f32 v117, v117\n"
    "v_mfma_f32_16x16x32_bf16 v[4:7], a[196:199], v[152:155], v[4:7]\n"
    "v_exp_f32 v118, v118\n"
    "v_mfma_f32_16x16x32_bf16 v[4:7], a[220:223], v[156:159], v[4:7]\n"
    "ds_read_b128 a[88:91], v183 offset:27712\n"
    "v_exp_f32 v119, v119\n"
    "v_mfma_f32_16x16x32_bf16 v[8:11], a[200:203], v[152:155], v[8:11]\n"
    "v_exp_f32 v120, v120\n"
    "v_mfma_f32_16x16x32_bf16 v[8:11], a[224:227], v[156:159], v[8:11]\n"
    "s_waitcnt lgkmcnt(3)\n"
    "v_exp_f32 v121, v121\n"
    "v_mfma_f32_16x16x32_bf16 v[12:15], a[204:207], v[152:155], v[12:15]\n"
    "v_exp_f32 v122, v122\n"
    "v_mfma_f32_16x16x32_bf16 v[12:15], a[228:231], v[156:159], v[12:15]\n"
    "v_exp_f32 v123, v123\n"
    "v_mfma_f32_16x16x32_bf16 v[16:19], a[208:211], v[152:155], v[16:19]\n"
    "ds_read_b128 a[92:95], v183 offset:27776\n"
    "v_exp_f32 v124, v124\n"
    "v_mfma_f32_16x16x32_bf16 v[16:19], a[232:235], v[156:159], v[16:19]\n"
    "v_exp_f32 v125, v125\n"
    "v_mfma_f32_16x16x32_bf16 v[20:23], a[212:215], v[152:155], v[20:23]\n"
    "v_exp_f32 v126, v126\n"
    "v_mfma_f32_16x16x32_bf16 v[20:23], a[236:239], v[156:159], v[20:23]\n"
    "v_exp_f32 v127, v127\n"
    "v_mfma_f32_16x16x32_bf16 v[128:131], a[96:99], a[24:27], 0\n"
    "ds_read_b64_tr_b16 a[144:145], v181 offset:24576\n"
    "v_cvt_pk_bf16_f32 v152, v112, v113\n"
    "v_mfma_f32_16x16x32_bf16 v[128:131], a[100:103], a[28:31], v[128:131]\n"
    "ds_read_b64_tr_b16 a[146:147], v181 offset:27648\n"
    "v_cvt_pk_bf16_f32 v153, v114, v115\n"
    "v_mfma_f32_16x16x32_bf16 v[128:131], a[104:107], a[32:35], v[128:131]\n"
    "ds_read_b64_tr_b16 a[168:169], v184 offset:24576\n"
    "v_cvt_pk_bf16_f32 v154, v116, v117\n"
    "v_mfma_f32_16x16x32_bf16 v[132:135], a[108:111], a[24:27], 0\n"
    "ds_read_b64_tr_b16 a[170:171], v184 offset:27648\n"
    "v_cvt_pk_bf16_f32 v155, v118, v119\n"
    "v_mfma_f32_16x16x32_bf16 v[132:135], a[112:115], a[28:31], v[132:135]\n"
    "ds_read_b64_tr_b16 a[148:149], v182 offset:24576\n"
    "v_cvt_pk_bf16_f32 v156, v120, v121\n"
    "v_mfma_f32_16x16x32_bf16 v[132:135], a[116:119], a[32:35], v[132:135]\n"
    "ds_read_b64_tr_b16 a[150:151], v182 offset:27648\n"
    "v_cvt_pk_bf16_f32 v157, v122, v123\n"
    "v_mfma_f32_16x16x32_bf16 v[136:139], a[120:123], a[24:27], 0\n"
    "ds_read_b64_tr_b16 a[172:173], v185 offset:24576\n"
    "v_cvt_pk_bf16_f32 v158, v124, v125\n"
    "v_mfma_f32_16x16x32_bf16 v[136:139], a[124:127], a[28:31], v[136:139]\n"
    "ds_read_b64_tr_b16 a[174:175], v185 offset:27648\n"
    "v_cvt_pk_bf16_f32 v159, v126, v127\n"
    "v_mfma_f32_16x16x32_bf16 v[136:139], a[128:131], a[32:35], v[136:139]\n"
    "v_exp_f32 v128, v128\n"
    "v_mfma_f32_16x16x32_bf16 v[140:143], a[132:135], a[24:27], 0\n"
    "v_exp_f32 v129, v129\n"
    "v_mfma_f32_16x16x32_bf16 v[140:143], a[136:139], a[28:31], v[140:143]\n"
    "v_exp_f32 v130, v130\n"
    "v_mfma_f32_16x16x32_bf16 v[140:143], a[140:143], a[32:35], v[140:143]\n"
    "v_exp_f32 v131, v131\n"
    "v_mfma_f32_16x16x32_bf16 v[144:147], a[120:123], a[36:39], v[164:167]\n"
    "v_exp_f32 v132, v132\n"
    "v_mfma_f32_16x16x32_bf16 v[144:147], a[124:127], a[40:43], v[144:147]\n"
    "v_exp_f32 v133, v133\n"
    "v_mfma_f32_16x16x32_bf16 v[144:147], a[128:131], a[44:47], v[144:147]\n"
    "v_exp_f32 v134, v134\n"
    "v_mfma_f32_16x16x32_bf16 v[148:151], a[132:135], a[36:39], v[164:167]\n"
    "v_exp_f32 v135, v135\n"
    "v_mfma_f32_16x16x32_bf16 v[148:151], a[136:139], a[40:43], v[148:151]\n"
    "v_exp_f32 v136, v136\n"
    "v_mfma_f32_16x16x32_bf16 v[148:151], a[140:143], a[44:47], v[148:151]\n"
    "v_exp_f32 v137, v137\n"
    "v_smfmac_f32_16x16x64_bf16 v[52:55], a[240:243], v[152:159], v239\n"
    "ds_read_b64_tr_b16 a[152:153], v181 offset:24640\n"
    "ds_read_b64_tr_b16 a[154:155], v181 offset:27712\n"
    "v_mfma_f32_16x16x32_bf16 v[28:31], a[192:195], v[152:155], v[28:31]\n"
    "v_exp_f32 v138, v138\n"
    "v_mfma_f32_16x16x32_bf16 v[28:31], a[216:219], v[156:159], v[28:31]\n"
    "v_exp_f32 v139, v139\n"
    "v_mfma_f32_16x16x32_bf16 v[32:35], a[196:199], v[152:155], v[32:35]\n"
    "v_exp_f32 v140, v140\n"
    "v_mfma_f32_16x16x32_bf16 v[32:35], a[220:223], v[156:159], v[32:35]\n"
    "v_exp_f32 v141, v141\n"
    "v_mfma_f32_16x16x32_bf16 v[36:39], a[200:203], v[152:155], v[36:39]\n"
    "v_exp_f32 v142, v142\n"
    "v_mfma_f32_16x16x32_bf16 v[36:39], a[224:227], v[156:159], v[36:39]\n"
    "v_exp_f32 v143, v143\n"
    "v_mfma_f32_16x16x32_bf16 v[40:43], a[204:207], v[152:155], v[40:43]\n"
    "v_exp_f32 v144, v144\n"
    "v_mfma_f32_16x16x32_bf16 v[40:43], a[228:231], v[156:159], v[40:43]\n"
    "v_exp_f32 v145, v145\n"
    "v_mfma_f32_16x16x32_bf16 v[44:47], a[208:211], v[152:155], v[44:47]\n"
    "v_exp_f32 v146, v146\n"
    "v_mfma_f32_16x16x32_bf16 v[44:47], a[232:235], v[156:159], v[44:47]\n"
    "v_exp_f32 v147, v147\n"
    "v_mfma_f32_16x16x32_bf16 v[48:51], a[212:215], v[152:155], v[48:51]\n"
    "v_exp_f32 v148, v148\n"
    "v_mfma_f32_16x16x32_bf16 v[48:51], a[236:239], v[156:159], v[48:51]\n"
    "v_exp_f32 v149, v149\n"
    "v_mfma_f32_16x16x32_bf16 v[112:115], a[48:51], a[0:3], 0\n"
    "ds_read_b64_tr_b16 a[176:177], v184 offset:24640\n"
    "v_cvt_pk_bf16_f32 v152, v128, v129\n"
    "v_mfma_f32_16x16x32_bf16 v[112:115], a[52:55], a[4:7], v[112:115]\n"
    "ds_read_b64_tr_b16 a[178:179], v184 offset:27712\n"
    "v_cvt_pk_bf16_f32 v153, v130, v131\n"
    "v_mfma_f32_16x16x32_bf16 v[112:115], a[56:59], a[8:11], v[112:115]\n"
    "ds_read_b64_tr_b16 a[156:157], v182 offset:24640\n"
    "v_cvt_pk_bf16_f32 v154, v132, v133\n"
    "v_mfma_f32_16x16x32_bf16 v[116:119], a[60:63], a[0:3], 0\n"
    "ds_read_b64_tr_b16 a[158:159], v182 offset:27712\n"
    "v_cvt_pk_bf16_f32 v155, v134, v135\n"
    "v_mfma_f32_16x16x32_bf16 v[116:119], a[64:67], a[4:7], v[116:119]\n"
    "ds_read_b64_tr_b16 a[180:181], v185 offset:24640\n"
    "v_cvt_pk_bf16_f32 v156, v136, v137\n"
    "v_mfma_f32_16x16x32_bf16 v[116:119], a[68:71], a[8:11], v[116:119]\n"
    "ds_read_b64_tr_b16 a[182:183], v185 offset:27712\n"
    "v_cvt_pk_bf16_f32 v157, v138, v139\n"
    "v_mfma_f32_16x16x32_bf16 v[120:123], a[72:75], a[0:3], 0\n"
    "ds_read_b64_tr_b16 a[160:161], v181 offset:24704\n"
    "v_cvt_pk_bf16_f32 v158, v140, v141\n"
    "v_mfma_f32_16x16x32_bf16 v[120:123], a[76:79], a[4:7], v[120:123]\n"
    "s_waitcnt lgkmcnt(6)\n"
    "ds_read_b64_tr_b16 a[162:163], v181 offset:27776\n"
    "v_cvt_pk_bf16_f32 v159, v142, v143\n"
    "v_mfma_f32_16x16x32_bf16 v[120:123], a[80:83], a[8:11], v[120:123]\n"
    "v_exp_f32 v150, v150\n"
    "v_mfma_f32_16x16x32_bf16 v[124:127], a[84:87], a[0:3], 0\n"
    "v_exp_f32 v151, v151\n"
    "v_mfma_f32_16x16x32_bf16 v[124:127], a[88:91], a[4:7], v[124:127]\n"
    "ds_read_b64_tr_b16 a[184:185], v184 offset:24704\n"
    "v_cvt_pk_bf16_f32 v160, v144, v145\n"
    "v_mfma_f32_16x16x32_bf16 v[124:127], a[92:95], a[8:11], v[124:127]\n"
    "ds_read_b64_tr_b16 a[186:187], v184 offset:27776\n"
    "v_cvt_pk_bf16_f32 v161, v146, v147\n"
    "v_smfmac_f32_16x16x64_bf16 v[80:83], a[240:243], v[152:159], v239\n"
    "ds_read_b64_tr_b16 a[164:165], v182 offset:24704\n"
    "v_cvt_pk_bf16_f32 v162, v148, v149\n"
    "v_mfma_f32_16x16x32_bf16 v[56:59], a[192:195], v[152:155], v[56:59]\n"
    "ds_read_b64_tr_b16 a[166:167], v182 offset:27776\n"
    "v_cvt_pk_bf16_f32 v163, v150, v151\n"
    "v_mfma_f32_16x16x32_bf16 v[56:59], a[216:219], v[156:159], v[56:59]\n"
    "v_exp_f32 v112, v112\n"
    "v_mfma_f32_16x16x32_bf16 v[60:63], a[196:199], v[152:155], v[60:63]\n"
    "v_exp_f32 v113, v113\n"
    "v_mfma_f32_16x16x32_bf16 v[60:63], a[220:223], v[156:159], v[60:63]\n"
    "v_exp_f32 v114, v114\n"
    "v_mfma_f32_16x16x32_bf16 v[64:67], a[200:203], v[152:155], v[64:67]\n"
    "ds_read_b64_tr_b16 a[188:189], v185 offset:24704\n"
    "ds_read_b64_tr_b16 a[190:191], v185 offset:27776\n"
    "v_mfma_f32_16x16x32_bf16 v[64:67], a[224:227], v[156:159], v[64:67]\n"
    "v_exp_f32 v115, v115\n"
    "v_mfma_f32_16x16x32_bf16 v[68:71], a[204:207], v[152:155], v[68:71]\n"
    "v_exp_f32 v116, v116\n"
    "v_mfma_f32_16x16x32_bf16 v[68:71], a[228:231], v[156:159], v[68:71]\n"
    "v_exp_f32 v117, v117\n"
    "v_mfma_f32_16x16x32_bf16 v[72:75], a[208:211], v[152:155], v[72:75]\n"
    "v_exp_f32 v118, v118\n"
    "v_mfma_f32_16x16x32_bf16 v[72:75], a[232:235], v[156:159], v[72:75]\n"
    "v_exp_f32 v119, v119\n"
    "v_mfma_f32_16x16x32_bf16 v[76:79], a[212:215], v[152:155], v[76:79]\n"
    "v_exp_f32 v120, v120\n"
    "v_mfma_f32_16x16x32_bf16 v[76:79], a[236:239], v[156:159], v[76:79]\n"
    "v_mfma_f32_16x16x32_bf16 v[84:87], a[216:219], v[160:163], v[84:87]\n"
    "v_exp_f32 v121, v121\n"
    "v_mfma_f32_16x16x32_bf16 v[88:91], a[220:223], v[160:163], v[88:91]\n"
    "v_exp_f32 v122, v122\n"
    "v_mfma_f32_16x16x32_bf16 v[92:95], a[224:227], v[160:163], v[92:95]\n"
    "v_exp_f32 v123, v123\n"
    "v_mfma_f32_16x16x32_bf16 v[96:99], a[228:231], v[160:163], v[96:99]\n"
    "v_exp_f32 v124, v124\n"
    "v_mfma_f32_16x16x32_bf16 v[100:103], a[232:235], v[160:163], v[100:103]\n"
    "v_exp_f32 v125, v125\n"
    "v_mfma_f32_16x16x32_bf16 v[104:107], a[236:239], v[160:163], v[104:107]\n"
    "v_exp_f32 v126, v126\n"
    "v_mfma_f32_16x16x32_bf16 v[108:111], a[240:243], v[160:163], v[108:111]\n"
    "v_exp_f32 v127, v127\n"
    "s_nop 0\n"
    "twins_p6:\n"
    "s_cbranch_execz twins_ret\n"
    "v_mfma_f32_16x16x32_bf16 v[128:131], a[48:51], a[12:15], 0\n"
    "ds_read_b128 a[96:99], v186 offset:24576\n"
    "v_cvt_pk_bf16_f32 v152, v112, v113\n"
    "v_mfma_f32_16x16x32_bf16 v[128:131], a[52:55], a[16:19], v[128:131]\n"
    "ds_read_b128 a[100:103], v186 offset:24640\n"
    "v_cvt_pk_bf16_f32 v153, v114, v115\n"
    "v_mfma_f32_16x16x32_bf16 v[128:131], a[56:59], a[20:23], v[128:131]\n"
    "ds_read_b128 a[104:107], v186 offset:24704\n"
    "v_cvt_pk_bf16_f32 v154, v116, v117\n"
    "v_mfma_f32_16x16x32_bf16 v[132:135], a[60:63], a[12:15], 0\n"
    "ds_read_b128 a[108:111], v186 offset:27648\n"
    "v_cvt_pk_bf16_f32 v155, v118, v119\n"
    "v_mfma_f32_16x16x32_bf16 v[132:135], a[64:67], a[16:19], v[132:135]\n"
    "ds_read_b128 a[112:115], v186 offset:27712\n"
    "v_cvt_pk_bf16_f32 v156, v120, v121\n"
    "v_mfma_f32_16x16x32_bf16 v[132:135], a[68:71], a[20:23], v[132:135]\n"
    "ds_read_b128 a[116:119], v186 offset:27776\n"
    "v_cvt_pk_bf16_f32 v157, v122, v123\n"
    "v_mfma_f32_16x16x32_bf16 v[136:139], a[72:75], a[12:15], 0\n"
    "ds_read_b128 a[120:123], v189 offset:24576\n"
    "v_cvt_pk_bf16_f32 v158, v124, v125\n"
    "v_mfma_f32_16x16x32_bf16 v[136:139], a[76:79], a[16:19], v[136:139]\n"
    "ds_read_b128 a[124:127], v189 offset:24640\n"
    "v_cvt_pk_bf16_f32 v159, v126, v127\n"
    "v_mfma_f32_16x16x32_bf16 v[136:139], a[80:83], a[20:23], v[136:139]\n"
    "v_exp_f32 v128, v128\n"
    "v_mfma_f32_16x16x32_bf16 v[140:143], a[84:87], a[12:15], 0\n"
    "v_exp_f32 v129, v129\n"
    "v_mfma_f32_16x16x32_bf16 v[140:143], a[88:91], a[16:19], v[140:143]\n"
    "v_exp_f32 v130, v130\n"
    "v_mfma_f32_16x16x32_bf16 v[140:143], a[92:95], a[20:23], v[140:143]\n"
    "v_exp_f32 v131, v131\n"
    "v_smfmac_f32_16x16x64_bf16 v[24:27], a[240:243], v[152:159], v239\n"
    "ds_read_b128 a[128:131], v189 offset:24704\n"
    "ds_read_b128 a[132:135], v189 offset:27648\n"
    "v_mfma_f32_16x16x32_bf16 v[0:3], a[144:147], v[152:155], v[0:3]\n"
    "v_exp_f32 v132, v132\n"
    "v_mfma_f32_16x16x32_bf16 v[0:3], a[168:171], v[156:159], v[0:3]\n"
    "v_exp_f32 v133, v133\n"
    "v_mfma_f32_16x16x32_bf16 v[4:7], a[148:151], v[152:155], v[4:7]\n"
    "v_exp_f32 v134, v134\n"
    "v_mfma_f32_16x16x32_bf16 v[4:7], a[172:175], v[156:159], v[4:7]\n"
    "ds_read_b128 a[136:139], v189 offset:27712\n"
    "v_exp_f32 v135, v135\n"
    "v_mfma_f32_16x16x32_bf16 v[8:11], a[152:155], v[152:155], v[8:11]\n"
    "s_waitcnt lgkmcnt(3)\n"
    "v_exp_f32 v136, v136\n"
    "v_mfma_f32_16x16x32_bf16 v[8:11], a[176:179], v[156:159], v[8:11]\n"
    "v_exp_f32 v137, v137\n"
    "v_mfma_f32_16x16x32_bf16 v[12:15], a[156:159], v[152:155], v[12:15]\n"
    "v_exp_f32 v138, v138\n"
    "v_mfma_f32_16x16x32_bf16 v[12:15], a[180:183], v[156:159], v[12:15]\n"
    "v_exp_f32 v139, v139\n"
    "v_mfma_f32_16x16x32_bf16 v[16:19], a[160:163], v[152:155], v[16:19]\n"
    "ds_read_b128 a[140:143], v189 offset:27776\n"
    "v_exp_f32 v140, v140\n"
    "v_mfma_f32_16x16x32_bf16 v[16:19], a[184:187], v[156:159], v[16:19]\n"
    "v_exp_f32 v141, v141\n"
    "v_mfma_f32_16x16x32_bf16 v[20:23], a[164:167], v[152:155], v[20:23]\n"
    "v_exp_f32 v142, v142\n"
    "v_mfma_f32_16x16x32_bf16 v[20:23], a[188:191], v[156:159], v[20:23]\n"
    "v_exp_f32 v143, v143\n"
    "v_mfma_f32_16x16x32_bf16 v[112:115], a[48:51], a[24:27], 0\n"
    "ds_read_b64_tr_b16 a[192:193], v187 offset:24576\n"
    "v_cvt_pk_bf16_f32 v152, v128, v129\n"
    "v_mfma_f32_16x16x32_bf16 v[112:115], a[52:55], a[28:31], v[112:115]\n"
    "ds_read_b64_tr_b16 a[194:195], v187 offset:27648\n"
    "v_cvt_pk_bf16_f32 v153, v130, v131\n"
    "v_mfma_f32_16x16x32_bf16 v[112:115], a[56:59], a[32:35], v[112:115]\n"
    "ds_read_b64_tr_b16 a[216:217], v190 offset:24576\n"
    "v_cvt_pk_bf16_f32 v154, v132, v133\n"
    "v_mfma_f32_16x16x32_bf16 v[116:119], a[60:63], a[24:27], 0\n"
    "ds_read_b64_tr_b16 a[218:219], v190 offset:27648\n"
    "v_cvt_pk_bf16_f32 v155, v134, v135\n"
    "v_mfma_f32_16x16x32_bf16 v[116:119], a[64:67], a[28:31], v[116:119]\n"
    "ds_read_b64_tr_b16 a[196:197], v188 offset:24576\n"
    "v_cvt_pk_bf16_f32 v156, v136, v137\n"
    "v_mfma_f32_16x16x32_bf16 v[116:119], a[68:71], a[32:35], v[116:119]\n"
    "ds_read_b64_tr_b16 a[198:199], v188 offset:27648\n"
    "v_cvt_pk_bf16_f32 v157, v138, v139\n"
    "v_mfma_f32_16x16x32_bf16 v[120:123], a[72:75], a[24:27], 0\n"
    "ds_read_b64_tr_b16 a[220:221], v191 offset:24576\n"
    "v_cvt_pk_bf16_f32 v158, v140, v141\n"
    "v_mfma_f32_16x16x32_bf16 v[120:123], a[76:79], a[28:31], v[120:123]\n"
    "ds_read_b64_tr_b16 a[222:223], v191 offset:27648\n"
    "v_cvt_pk_bf16_f32 v159, v142, v143\n"
    "v_mfma_f32_16x16x32_bf16 v[120:123], a[80:83], a[32:35], v[120:123]\n"
    "v_exp_f32 v112, v112\n"
    "v_mfma_f32_16x16x32_bf16 v[124:127], a[84:87], a[24:27], 0\n"
    "v_exp_f32 v113, v113\n"
    "v_mfma_f32_16x16x32_bf16 v[124:127], a[88:91], a[28:31], v[124:127]\n"
    "v_exp_f32 v114, v114\n"
    "v_mfma_f32_16x16x32_bf16 v[124:127], a[92:95], a[32:35], v[124:127]\n"
    "v_exp_f32 v115, v115\n"
    "v_mfma_f32_16x16x32_bf16 v[144:147], a[72:75], a[36:39], v[164:167]\n"
    "v_exp_f32 v116, v116\n"
    "v_mfma_f32_16x16x32_bf16 v[144:147], a[76:79], a[40:43], v[144:147]\n"
    "v_exp_f32 v117, v117\n"
    "v_mfma_f32_16x16x32_bf16 v[144:147], a[80:83], a[44:47], v[144:147]\n"
    "v_exp_f32 v118, v118\n"
    "v_mfma_f32_16x16x32_bf16 v[148:151], a[84:87], a[36:39], v[164:167]\n"
    "v_exp_f32 v119, v119\n"
    "v_mfma_f32_16x16x32_bf16 v[148:151], a[88:91], a[40:43], v[148:151]\n"
    "v_exp_f32 v120, v120\n"
    "v_mfma_f32_16x16x32_bf16 v[148:151], a[92:95], a[44:47], v[148:151]\n"
    "v_exp_f32 v121, v121\n"
    "v_smfmac_f32_16x16x64_bf16 v[52:55], a[240:243], v[152:159], v239\n"
    "ds_read_b64_tr_b16 a[200:201], v187 offset:24640\n"
    "ds_read_b64_tr_b16 a[202:203], v187 offset:27712\n"
    "v_mfma_f32_16x16x32_bf16 v[28:31], a[144:147], v[152:155], v[28:31]\n"
    "v_exp_f32 v122, v122\n"
    "v_mfma_f32_16x16x32_bf16 v[28:31], a[168:171], v[156:159], v[28:31]\n"
    "v_exp_f32 v123, v123\n"
    "v_mfma_f32_16x16x32_bf16 v[32:35], a[148:151], v[152:155], v[32:35]\n"
    "v_exp_f32 v124, v124\n"
    "v_mfma_f32_16x16x32_bf16 v[32:35], a[172:175], v[156:159], v[32:35]\n"
    "v_exp_f32 v125, v125\n"
    "v_mfma_f32_16x16x32_bf16 v[36:39], a[152:155], v[152:155], v[36:39]\n"
    "v_exp_f32 v126, v126\n"
    "v_mfma_f32_16x16x32_bf16 v[36:39], a[176:179], v[156:159], v[36:39]\n"
    "v_exp_f32 v127, v127\n"
    "v_mfma_f32_16x16x32_bf16 v[40:43], a[156:159], v[152:155], v[40:43]\n"
    "v_exp_f32 v144, v144\n"
    "v_mfma_f32_16x16x32_bf16 v[40:43], a[180:183], v[156:159], v[40:43]\n"
    "v_exp_f32 v145, v145\n"
    "v_mfma_f32_16x16x32_bf16 v[44:47], a[160:163], v[152:155], v[44:47]\n"
    "v_exp_f32 v146, v146\n"
    "v_mfma_f32_16x16x32_bf16 v[44:47], a[184:187], v[156:159], v[44:47]\n"
    "v_exp_f32 v147, v147\n"
    "v_mfma_f32_16x16x32_bf16 v[48:51], a[164:167], v[152:155], v[48:51]\n"
    "v_exp_f32 v148, v148\n"
    "v_mfma_f32_16x16x32_bf16 v[48:51], a[188:191], v[156:159], v[48:51]\n"
    "v_exp_f32 v149, v149\n"
    "v_mfma_f32_16x16x32_bf16 v[128:131], a[96:99], a[0:3], 0\n"
    "ds_read_b64_tr_b16 a[224:225], v190 offset:24640\n"
    "v_cvt_pk_bf16_f32 v152, v112, v113\n"
    "v_mfma_f32_16x16x32_bf16 v[128:131], a[100:103], a[4:7], v[128:131]\n"
    "ds_read_b64_tr_b16 a[226:227], v190 offset:27712\n"
    "v_cvt_pk_bf16_f32 v153, v114, v115\n"
    "v_mfma_f32_16x16x32_bf16 v[128:131], a[104:107], a[8:11], v[128:131]\n"
    "ds_read_b64_tr_b16 a[204:205], v188 offset:24640\n"
    "v_cvt_pk_bf16_f32 v154, v116, v117\n"
    "v_mfma_f32_16x16x32_bf16 v[132:135], a[108:111], a[0:3], 0\n"
    "ds_read_b64_tr_b16 a[206:207], v188 offset:27712\n"
    "v_cvt_pk_bf16_f32 v155, v118, v119\n"
    "v_mfma_f32_16x16x32_bf16 v[132:135], a[112:115], a[4:7], v[132:135]\n"
    "ds_read_b64_tr_b16 a[228:229], v191 offset:24640\n"
    "v_cvt_pk_bf16_f32 v156, v120, v121\n"
    "v_mfma_f32_16x16x32_bf16 v[132:135], a[116:119], a[8:11], v[132:135]\n"
    "ds_read_b64_tr_b16 a[230:231], v191 offset:27712\n"
    "v_cvt_pk_bf16_f32 v157, v122, v123\n"
    "v_mfma_f32_16x16x32_bf16 v[136:139], a[120:123], a[0:3], 0\n"
    "ds_read_b64_tr_b16 a[208:209], v187 offset:24704\n"
    "v_cvt_pk_bf16_f32 v158, v124, v125\n"
    "v_mfma_f32_16x16x32_bf16 v[136:139], a[124:127], a[4:7], v[136:139]\n"
    "s_waitcnt lgkmcnt(6)\n"
    "ds_read_b64_tr_b16 a[210:211], v187 offset:27776\n"
    "v_cvt_pk_bf16_f32 v159, v126, v127\n"
    "v_mfma_f32_16x16x32_bf16 v[136:139], a[128:131], a[8:11], v[136:139]\n"
    "v_exp_f32 v150, v150\n"
    "v_mfma_f32_16x16x32_bf16 v[140:143], a[132:135], a[0:3], 0\n"
    "v_exp_f32 v151, v151\n"
    "v_mfma_f32_16x16x32_bf16 v[140:143], a[136:139], a[4:7], v[140:143]\n"
    "ds_read_b64_tr_b16 a[232:233], v190 offset:24704\n"
    "v_cvt_pk_bf16_f32 v160, v144, v145\n"
    "v_mfma_f32_16x16x32_bf16 v[140:143], a[140:143], a[8:11], v[140:143]\n"
    "ds_read_b64_tr_b16 a[234:235], v190 offset:27776\n"
    "v_cvt_pk_bf16_f32 v161, v146, v147\n"
    "v_smfmac_f32_16x16x64_bf16 v[80:83], a[240:243], v[152:159], v239\n"
    "ds_read_b64_tr_b16 a[212:213], v188 offset:24704\n"
    "v_cvt_pk_bf16_f32 v162, v148, v149\n"
    "v_mfma_f32_16x16x32_bf16 v[56:59], a[144:147], v[152:155], v[56:59]\n"
    "ds_read_b64_tr_b16 a[214:215], v188 offset:27776\n"
    "v_cvt_pk_bf16_f32 v163, v150, v151\n"
    "v_mfma_f32_16x16x32_bf16 v[56:59], a[168:171], v[156:159], v[56:59]\n"
    "v_exp_f32 v128, v128\n"
    "v_mfma_f32_16x16x32_bf16 v[60:63], a[148:151], v[152:155], v[60:63]\n"
    "ds_read_b128 a[48:51], v238 offset:0\n"
    "v_exp_f32 v129, v129\n"
    "v_mfma_f32_16x16x32_bf16 v[60:63], a[172:175], v[156:159], v[60:63]\n"
    "v_exp_f32 v130, v130\n"
    "v_mfma_f32_16x16x32_bf16 v[64:67], a[152:155], v[152:155], v[64:67]\n"
    "ds_read_b64_tr_b16 a[236:237], v191 offset:24704\n"
    "ds_read_b128 a[52:55], v238 offset:3072\n"
    "v_mfma_f32_16x16x32_bf16 v[64:67], a[176:179], v[156:159], v[64:67]\n"
    "v_exp_f32 v131, v131\n"
    "v_mfma_f32_16x16x32_bf16 v[68:71], a[156:159], v[152:155], v[68:71]\n"
    "v_exp_f32 v132, v132\n"
    "v_mfma_f32_16x16x32_bf16 v[68:71], a[180:183], v[156:159], v[68:71]\n"
    "ds_read_b128 a[56:59], v238 offset:6144\n"
    "v_exp_f32 v133, v133\n"
    "v_mfma_f32_16x16x32_bf16 v[72:75], a[160:163], v[152:155], v[72:75]\n"
    "v_exp_f32 v134, v134\n"
    "v_mfma_f32_16x16x32_bf16 v[72:75], a[184:187], v[156:159], v[72:75]\n"
    "v_exp_f32 v135, v135\n"
    "v_mfma_f32_16x16x32_bf16 v[76:79], a[164:167], v[152:155], v[76:79]\n"
    "ds_read_b64_tr_b16 a[238:239], v191 offset:27776\n"
    "ds_read_b128 a[60:63], v238 offset:9216\n"
    "v_mfma_f32_16x16x32_bf16 v[76:79], a[188:191], v[156:159], v[76:79]\n"
    "v_exp_f32 v136, v136\n"
    "v_mfma_f32_16x16x32_bf16 v[84:87], a[168:171], v[160:163], v[84:87]\n"
    "v_exp_f32 v137, v137\n"
    "v_mfma_f32_16x16x32_bf16 v[88:91], a[172:175], v[160:163], v[88:91]\n"
    "ds_read_b128 a[64:67], v238 offset:12288\n"
    "v_exp_f32 v138, v138\n"
    "v_mfma_f32_16x16x32_bf16 v[92:95], a[176:179], v[160:163], v[92:95]\n"
    "v_exp_f32 v139, v139\n"
    "v_mfma_f32_16x16x32_bf16 v[96:99], a[180:183], v[160:163], v[96:99]\n"
    "v_exp_f32 v140, v140\n"
    "v_mfma_f32_16x16x32_bf16 v[100:103], a[184:187], v[160:163], v[100:103]\n"
    "ds_read_b128 a[68:71], v238 offset:15360\n"
    "v_exp_f32 v141, v141\n"
    "v_mfma_f32_16x16x32_bf16 v[104:107], a[188:191], v[160:163], v[104:107]\n"
    "v_exp_f32 v142, v142\n"
    "v_mfma_f32_16x16x32_bf16 v[108:111], a[240:243], v[160:163], v[108:111]\n"
    "v_exp_f32 v143, v143\n"
    "v_mfma_f32_16x16x32_bf16 v[112:115], a[96:99], a[12:15], 0\n"
    "ds_read_b128 a[72:75], v238 offset:1024\n"
    "v_cvt_pk_bf16_f32 v152, v128, v129\n"
    "v_cvt_pk_bf16_f32 v153, v130, v131\n"
    "v_mfma_f32_16x16x32_bf16 v[112:115], a[100:103], a[16:19], v[112:115]\n"
    "ds_read_b128 a[76:79], v238 offset:4096\n"
    "v_cvt_pk_bf16_f32 v154, v132, v133\n"
    "v_cvt_pk_bf16_f32 v155, v134, v135\n"
    "v_mfma_f32_16x16x32_bf16 v[112:115], a[104:107], a[20:23], v[112:115]\n"
    "ds_read_b128 a[80:83], v238 offset:7168\n"
    "v_cvt_pk_bf16_f32 v156, v136, v137\n"
    "v_cvt_pk_bf16_f32 v157, v138, v139\n"
    "v_mfma_f32_16x16x32_bf16 v[116:119], a[108:111], a[12:15], 0\n"
    "ds_read_b128 a[84:87], v238 offset:10240\n"
    "v_cvt_pk_bf16_f32 v158, v140, v141\n"
    "v_cvt_pk_bf16_f32 v159, v142, v143\n"
    "v_mfma_f32_16x16x32_bf16 v[116:119], a[112:115], a[16:19], v[116:119]\n"
    "ds_read_b128 a[88:91], v238 offset:13312\n"
    "v_cvt_pk_bf16_f32 v192, v84, v85\n"
    "v_cvt_pk_bf16_f32 v193, v86, v87\n"
    "v_mfma_f32_16x16x32_bf16 v[116:119], a[116:119], a[20:23], v[116:119]\n"
    "ds_read_b128 a[92:95], v238 offset:16384\n"
    "v_cvt_pk_bf16_f32 v194, v88, v89\n"
    "v_cvt_pk_bf16_f32 v195, v90, v91\n"
    "v_mfma_f32_16x16x32_bf16 v[120:123], a[120:123], a[12:15], 0\n"
    "ds_read_b128 a[144:147], v238 offset:2048\n"
    "v_cvt_pk_bf16_f32 v196, v92, v93\n"
    "v_cvt_pk_bf16_f32 v197, v94, v95\n"
    "v_mfma_f32_16x16x32_bf16 v[120:123], a[124:127], a[16:19], v[120:123]\n"
    "ds_read_b128 a[148:151], v238 offset:5120\n"
    "v_cvt_pk_bf16_f32 v198, v96, v97\n"
    "v_cvt_pk_bf16_f32 v199, v98, v99\n"
    "v_mfma_f32_16x16x32_bf16 v[120:123], a[128:131], a[20:23], v[120:123]\n"
    "ds_read_b128 a[152:155], v238 offset:8192\n"
    "v_cvt_pk_bf16_f32 v200, v100, v101\n"
    "v_cvt_pk_bf16_f32 v201, v102, v103\n"
    "v_mfma_f32_16x16x32_bf16 v[124:127], a[132:135], a[12:15], 0\n"
    "v_exp_f32 v112, v112\n"
    "v_mfma_f32_16x16x32_bf16 v[124:127], a[136:139], a[16:19], v[124:127]\n"
    "v_exp_f32 v113, v113\n"
    "v_mfma_f32_16x16x32_bf16 v[124:127], a[140:143], a[20:23], v[124:127]\n"
    "v_exp_f32 v114, v114\n"
    "v_smfmac_f32_16x16x64_bf16 v[24:27], a[240:243], v[152:159], v239\n"
    "v_exp_f32 v115, v115\n"
    "v_mfma_f32_16x16x32_bf16 v[0:3], a[192:195], v[152:155], v[0:3]\n"
    "ds_write_b128 v229, v[108:111] offset:6144\n"
    "v_mfma_f32_16x16x32_bf16 v[0:3], a[216:219], v[156:159], v[0:3]\n"
    "v_exp_f32 v116, v116\n"
    "v_mfma_f32_16x16x32_bf16 v[4:7], a[196:199], v[152:155], v[4:7]\n"
    "v_exp_f32 v117, v117\n"
    "v_mfma_f32_16x16x32_bf16 v[4:7], a[220:223], v[156:159], v[4:7]\n"
    "v_exp_f32 v118, v118\n"
    "v_mfma_f32_16x16x32_bf16 v[8:11], a[200:203], v[152:155], v[8:11]\n"
    "s_waitcnt lgkmcnt(1)\n"
    "v_exp_f32 v119, v119\n"
    "v_mfma_f32_16x16x32_bf16 v[8:11], a[224:227], v[156:159], v[8:11]\n"
    "v_exp_f32 v120, v120\n"
    "v_mfma_f32_16x16x32_bf16 v[12:15], a[204:207], v[152:155], v[12:15]\n"
    "v_exp_f32 v121, v121\n"
    "v_mfma_f32_16x16x32_bf16 v[12:15], a[228:231], v[156:159], v[12:15]\n"
    "ds_read_b128 a[156:159], v238 offset:11264\n"
    "v_exp_f32 v122, v122\n"
    "v_mfma_f32_16x16x32_bf16 v[16:19], a[208:211], v[152:155], v[16:19]\n"
    "ds_read_b128 a[160:163], v238 offset:14336\n"
    "v_exp_f32 v123, v123\n"
    "v_mfma_f32_16x16x32_bf16 v[16:19], a[232:235], v[156:159], v[16:19]\n"
    "ds_read_b128 a[164:167], v238 offset:17408\n"
    "v_exp_f32 v124, v124\n"
    "v_mfma_f32_16x16x32_bf16 v[20:23], a[212:215], v[152:155], v[20:23]\n"
    "v_exp_f32 v125, v125\n"
    "v_mfma_f32_16x16x32_bf16 v[20:23], a[236:239], v[156:159], v[20:23]\n"
    "v_exp_f32 v126, v126\n"
    "v_mfma_f32_16x16x32_bf16 v[168:171], a[48:51], v[192:195], 0\n"
    "v_exp_f32 v127, v127\n"
    "v_mfma_f32_16x16x32_bf16 v[172:175], a[52:55], v[192:195], 0\n"
    "v_cvt_pk_bf16_f32 v202, v104, v105\n"
    "v_add_f32 v24, v24, v25\n"
    "v_cvt_pk_bf16_f32 v203, v106, v107\n"
    "v_mfma_f32_16x16x32_bf16 v[176:179], a[56:59], v[192:195], 0\n"
    "v_rcp_f32 v217, v24\n"
    "v_mfma_f32_16x16x32_bf16 v[180:183], a[60:63], v[192:195], 0\n"
    "v_mul_f32 v0, v217, v0\n"
    "v_mul_f32 v1, v217, v1\n"
    "v_cvt_pk_bf16_f32 v144, v0, v1\n"
    "v_mfma_f32_16x16x32_bf16 v[184:187], a[64:67], v[192:195], 0\n"
    "v_mul_f32 v2, v217, v2\n"
    "v_mul_f32 v3, v217, v3\n"
    "v_cvt_pk_bf16_f32 v145, v2, v3\n"
    "v_mfma_f32_16x16x32_bf16 v[188:191], a[68:71], v[192:195], 0\n"
    "v_mul_f32 v4, v217, v4\n"
    "v_mul_f32 v5, v217, v5\n"
    "v_cvt_pk_bf16_f32 v146, v4, v5\n"
    "v_mfma_f32_16x16x32_bf16 v[168:171], a[72:75], v[196:199], v[168:171]\n"
    "v_mul_f32 v6, v217, v6\n"
    "v_mul_f32 v7, v217, v7\n"
    "v_cvt_pk_bf16_f32 v147, v6, v7\n"
    "v_mfma_f32_16x16x32_bf16 v[172:175], a[76:79], v[196:199], v[172:175]\n"
    "v_mul_f32 v8, v217, v8\n"
    "v_mul_f32 v9, v217, v9\n"
    "v_mfma_f32_16x16x32_bf16 v[176:179], a[80:83], v[196:199], v[176:179]\n"
    "v_cvt_pk_bf16_f32 v148, v8, v9\n"
    "v_mul_f32 v10, v217, v10\n"
    "v_mfma_f32_16x16x32_bf16 v[180:183], a[84:87], v[196:199], v[180:183]\n"
    "v_mul_f32 v11, v217, v11\n"
    "v_cvt_pk_bf16_f32 v152, v112, v113\n"
    "v_mfma_f32_16x16x32_bf16 v[184:187], a[88:91], v[196:199], v[184:187]\n"
    "v_cvt_pk_bf16_f32 v149, v10, v11\n"
    "v_cvt_pk_bf16_f32 v153, v114, v115\n"
    "v_mfma_f32_16x16x32_bf16 v[188:191], a[92:95], v[196:199], v[188:191]\n"
    "v_cvt_pk_bf16_f32 v154, v116, v117\n"
    "v_cvt_pk_bf16_f32 v155, v118, v119\n"
    "v_mfma_f32_16x16x32_bf16 v[168:171], a[144:147], v[200:203], v[168:171]\n"
    "v_cvt_pk_bf16_f32 v156, v120, v121\n"
    "v_cvt_pk_bf16_f32 v157, v122, v123\n"
    "v_mfma_f32_16x16x32_bf16 v[172:175], a[148:151], v[200:203], v[172:175]\n"
    "v_cvt_pk_bf16_f32 v158, v124, v125\n"
    "v_cvt_pk_bf16_f32 v159, v126, v127\n"
    "v_mfma_f32_16x16x32_bf16 v[176:179], a[152:155], v[200:203], v[176:179]\n"
    "s_waitcnt lgkmcnt(0)\n"
    "v_mul_f32 v12, v217, v12\n"
    "v_mul_f32 v13, v217, v13\n"
    "v_mfma_f32_16x16x32_bf16 v[180:183], a[156:159], v[200:203], v[180:183]\n"
    "v_cvt_pk_bf16_f32 v150, v12, v13\n"
    "v_mul_f32 v14, v217, v14\n"
    "v_mfma_f32_16x16x32_bf16 v[184:187], a[160:163], v[200:203], v[184:187]\n"
    "v_mul_f32 v15, v217, v15\n"
    "v_cvt_pk_bf16_f32 v151, v14, v15\n"
    "v_mfma_f32_16x16x32_bf16 v[188:191], a[164:167], v[200:203], v[188:191]\n"
    "v_mul_f32 v16, v217, v16\n"
    "v_mul_f32 v17, v217, v17\n"
    "v_mfma_f32_16x16x32_bf16 v[128:131], a[96:99], a[24:27], 0\n"
    "v_cvt_pk_bf16_f32 v160, v16, v17\n"
    "v_mul_f32 v18, v217, v18\n"
    "v_mfma_f32_16x16x32_bf16 v[128:131], a[100:103], a[28:31], v[128:131]\n"
    "v_mul_f32 v19, v217, v19\n"
    "v_cvt_pk_bf16_f32 v161, v18, v19\n"
    "v_mfma_f32_16x16x32_bf16 v[128:131], a[104:107], a[32:35], v[128:131]\n"
    "v_mul_f32 v20, v217, v20\n"
    "v_mul_f32 v21, v217, v21\n"
    "v_mfma_f32_16x16x32_bf16 v[132:135], a[108:111], a[24:27], 0\n"
    "v_cvt_pk_bf16_f32 v162, v20, v21\n"
    "v_mul_f32 v22, v217, v22\n"
    "v_mfma_f32_16x16x32_bf16 v[132:135], a[112:115], a[28:31], v[132:135]\n"
    "v_mul_f32 v23, v217, v23\n"
    "v_cvt_pk_bf16_f32 v163, v22, v23\n"
    "v_mfma_f32_16x16x32_bf16 v[132:135], a[116:119], a[32:35], v[132:135]\n"
    "ds_write_b128 v229, v[168:171] offset:0\n"
    "v_mfma_f32_16x16x32_bf16 v[136:139], a[120:123], a[24:27], 0\n"
    "ds_write_b128 v229, v[172:175] offset:1024\n"
    "v_mfma_f32_16x16x32_bf16 v[136:139], a[124:127], a[28:31], v[136:139]\n"
    "ds_write_b128 v229, v[176:179] offset:2048\n"
    "v_mfma_f32_16x16x32_bf16 v[136:139], a[128:131], a[32:35], v[136:139]\n"
    "ds_write_b128 v229, v[180:183] offset:3072\n"
    "v_mfma_f32_16x16x32_bf16 v[140:143], a[132:135], a[24:27], 0\n"
    "ds_write_b128 v229, v[184:187] offset:4096\n"
    "v_mfma_f32_16x16x32_bf16 v[140:143], a[136:139], a[28:31], v[140:143]\n"
    "ds_write_b128 v229, v[188:191] offset:5120\n"
    "v_mfma_f32_16x16x32_bf16 v[140:143], a[140:143], a[32:35], v[140:143]\n"
    "v_exp_f32 v128, v128\n"
    "v_mfma_f32_16x16x32_bf16 v[84:87], a[48:51], v[144:147], 0\n"
    "v_exp_f32 v129, v129\n"
    "v_mfma_f32_16x16x32_bf16 v[88:91], a[52:55], v[144:147], 0\n"
    "v_exp_f32 v130, v130\n"
    "v_mfma_f32_16x16x32_bf16 v[92:95], a[56:59], v[144:147], 0\n"
    "v_exp_f32 v131, v131\n"
    "v_mfma_f32_16x16x32_bf16 v[96:99], a[60:63], v[144:147], 0\n"
    "v_cvt_pk_bf16_f32 v112, v128, v129\n"
    "v_cvt_pk_bf16_f32 v113, v130, v131\n"
    "v_mfma_f32_16x16x32_bf16 v[100:103], a[64:67], v[144:147], 0\n"
    "v_exp_f32 v132, v132\n"
    "v_mfma_f32_16x16x32_bf16 v[104:107], a[68:71], v[144:147], 0\n"
    "v_exp_f32 v133, v133\n"
    "v_mfma_f32_16x16x32_bf16 v[84:87], a[72:75], v[148:151], v[84:87]\n"
    "v_exp_f32 v134, v134\n"
    "v_mfma_f32_16x16x32_bf16 v[88:91], a[76:79], v[148:151], v[88:91]\n"
    "v_exp_f32 v135, v135\n"
    "v_mfma_f32_16x16x32_bf16 v[92:95], a[80:83], v[148:151], v[92:95]\n"
    "v_cvt_pk_bf16_f32 v114, v132, v133\n"
    "v_cvt_pk_bf16_f32 v115, v134, v135\n"
    "v_mfma_f32_16x16x32_bf16 v[96:99], a[84:87], v[148:151], v[96:99]\n"
    "v_exp_f32 v136, v136\n"
    "v_mfma_f32_16x16x32_bf16 v[100:103], a[88:91], v[148:151], v[100:103]\n"
    "v_exp_f32 v137, v137\n"
    "v_mfma_f32_16x16x32_bf16 v[104:107], a[92:95], v[148:151], v[104:107]\n"
    "v_exp_f32 v138, v138\n"
    "v_mfma_f32_16x16x32_bf16 v[84:87], a[144:147], v[160:163], v[84:87]\n"
    "v_exp_f32 v139, v139\n"
    "v_mfma_f32_16x16x32_bf16 v[88:91], a[148:151], v[160:163], v[88:91]\n"
    "v_cvt_pk_bf16_f32 v116, v136, v137\n"
    "v_cvt_pk_bf16_f32 v117, v138, v139\n"
    "v_mfma_f32_16x16x32_bf16 v[92:95], a[152:155], v[160:163], v[92:95]\n"
    "v_exp_f32 v140, v140\n"
    "v_mfma_f32_16x16x32_bf16 v[96:99], a[156:159], v[160:163], v[96:99]\n"
    "v_exp_f32 v141, v141\n"
    "v_mfma_f32_16x16x32_bf16 v[100:103], a[160:163], v[160:163], v[100:103]\n"
    "v_exp_f32 v142, v142\n"
    "v_mfma_f32_16x16x32_bf16 v[104:107], a[164:167], v[160:163], v[104:107]\n"
    "v_exp_f32 v143, v143\n"
    "v_smfmac_f32_16x16x64_bf16 v[52:55], a[240:243], v[152:159], v239\n"
    "v_cvt_pk_bf16_f32 v118, v140, v141\n"
    "v_cvt_pk_bf16_f32 v204, v84, v85\n"
    "v_mfma_f32_16x16x32_bf16 v[28:31], a[192:195], v[152:155], v[28:31]\n"
    "v_cvt_pk_bf16_f32 v205, v86, v87\n"
    "v_cvt_pk_bf16_f32 v206, v88, v89\n"
    "v_mfma_f32_16x16x32_bf16 v[28:31], a[216:219], v[156:159], v[28:31]\n"
    "v_cvt_pk_bf16_f32 v207, v90, v91\n"
    "v_cvt_pk_bf16_f32 v208, v92, v93\n"
    "v_mfma_f32_16x16x32_bf16 v[32:35], a[196:199], v[152:155], v[32:35]\n"
    "global_store_dwordx4 v232, v[204:207], s[86:87] offset:0 sc1\n"
    "v_mfma_f32_16x16x32_bf16 v[32:35], a[220:223], v[156:159], v[32:35]\n"
    "v_cvt_pk_bf16_f32 v209, v94, v95\n"
    "v_cvt_pk_bf16_f32 v210, v96, v97\n"
    "v_mfma_f32_16x16x32_bf16 v[36:39], a[200:203], v[152:155], v[36:39]\n"
    "v_cvt_pk_bf16_f32 v211, v98, v99\n"
    "v_cvt_pk_bf16_f32 v119, v142, v143\n"
    "v_mfma_f32_16x16x32_bf16 v[36:39], a[224:227], v[156:159], v[36:39]\n"
    "global_store_dwordx4 v232, v[208:211], s[86:87] offset:64 sc1\n"
    "v_mfma_f32_16x16x32_bf16 v[40:43], a[204:207], v[152:155], v[40:43]\n"
    "v_cvt_pk_bf16_f32 v212, v100, v101\n"
    "v_cvt_pk_bf16_f32 v213, v102, v103\n"
    "v_mfma_f32_16x16x32_bf16 v[40:43], a[228:231], v[156:159], v[40:43]\n"
    "v_cvt_pk_bf16_f32 v214, v104, v105\n"
    "v_add_f32 v52, v52, v53\n"
    "v_mfma_f32_16x16x32_bf16 v[44:47], a[208:211], v[152:155], v[44:47]\n"
    "v_cvt_pk_bf16_f32 v215, v106, v107\n"
    "v_cmp_nle_f32 s[64:65], s60, v24\n"
    "v_mfma_f32_16x16x32_bf16 v[44:47], a[232:235], v[156:159], v[44:47]\n"
    "global_store_dwordx4 v232, v[212:215], s[86:87] offset:128 sc1\n"
    "v_mfma_f32_16x16x32_bf16 v[48:51], a[212:215], v[152:155], v[48:51]\n"
    "v_rcp_f32 v218, v52\n"
    "v_mfma_f32_16x16x32_bf16 v[48:51], a[236:239], v[156:159], v[48:51]\n"
    "v_mul_f32 v28, v218, v28\n"
    "v_mul_f32 v29, v218, v29\n"
    "v_smfmac_f32_16x16x64_bf16 v[80:83], a[240:243], v[112:119], v239\n"
    "v_cvt_pk_bf16_f32 v192, v28, v29\n"
    "v_mul_f32 v30, v218, v30\n"
    "v_mfma_f32_16x16x32_bf16 v[56:59], a[192:195], v[112:115], v[56:59]\n"
    "v_mul_f32 v31, v218, v31\n"
    "v_cvt_pk_bf16_f32 v193, v30, v31\n"
    "v_mfma_f32_16x16x32_bf16 v[56:59], a[216:219], v[116:119], v[56:59]\n"
    "v_mul_f32 v32, v218, v32\n"
    "v_mul_f32 v33, v218, v33\n"
    "v_mfma_f32_16x16x32_bf16 v[60:63], a[196:199], v[112:115], v[60:63]\n"
    "v_cvt_pk_bf16_f32 v194, v32, v33\n"
    "v_mul_f32 v34, v218, v34\n"
    "v_mfma_f32_16x16x32_bf16 v[60:63], a[220:223], v[116:119], v[60:63]\n"
    "s_waitcnt lgkmcnt(0)\n"
    "v_mul_f32 v35, v218, v35\n"
    "v_cvt_pk_bf16_f32 v195, v34, v35\n"
    "v_mfma_f32_16x16x32_bf16 v[64:67], a[200:203], v[112:115], v[64:67]\n"
    "s_barrier\n"
    "v_mul_f32 v36, v218, v36\n"
    "v_mul_f32 v37, v218, v37\n"
    "v_mfma_f32_16x16x32_bf16 v[64:67], a[224:227], v[116:119], v[64:67]\n"
    "v_cvt_pk_bf16_f32 v196, v36, v37\n"
    "v_mul_f32 v38, v218, v38\n"
    "v_mfma_f32_16x16x32_bf16 v[68:71], a[204:207], v[112:115], v[68:71]\n"
    "v_mul_f32 v39, v218, v39\n"
    "v_cvt_pk_bf16_f32 v197, v38, v39\n"
    "v_mfma_f32_16x16x32_bf16 v[68:71], a[228:231], v[116:119], v[68:71]\n"
    "v_mul_f32 v40, v218, v40\n"
    "v_mul_f32 v41, v218, v41\n"
    "v_mfma_f32_16x16x32_bf16 v[72:75], a[208:211], v[112:115], v[72:75]\n"
    "v_cvt_pk_bf16_f32 v198, v40, v41\n"
    "v_mul_f32 v42, v218, v42\n"
    "v_mfma_f32_16x16x32_bf16 v[72:75], a[232:235], v[116:119], v[72:75]\n"
    "v_mul_f32 v43, v218, v43\n"
    "v_cvt_pk_bf16_f32 v199, v42, v43\n"
    "v_mfma_f32_16x16x32_bf16 v[76:79], a[212:215], v[112:115], v[76:79]\n"
    "v_mul_f32 v44, v218, v44\n"
    "v_mul_f32 v45, v218, v45\n"
    "v_mfma_f32_16x16x32_bf16 v[76:79], a[236:239], v[116:119], v[76:79]\n"
    "v_cvt_pk_bf16_f32 v200, v44, v45\n"
    "v_mul_f32 v46, v218, v46\n"
    "v_mfma_f32_16x16x32_bf16 v[0:3], a[48:51], v[192:195], 0\n"
    "v_mul_f32 v47, v218, v47\n"
    "v_cvt_pk_bf16_f32 v201, v46, v47\n"
    "v_mfma_f32_16x16x32_bf16 v[4:7], a[52:55], v[192:195], 0\n"
    "v_mul_f32 v48, v218, v48\n"
    "v_mul_f32 v49, v218, v49\n"
    "v_mfma_f32_16x16x32_bf16 v[8:11], a[56:59], v[192:195], 0\n"
    "v_cvt_pk_bf16_f32 v202, v48, v49\n"
    "v_mul_f32 v50, v218, v50\n"
    "v_mfma_f32_16x16x32_bf16 v[12:15], a[60:63], v[192:195], 0\n"
    "v_mul_f32 v51, v218, v51\n"
    "v_cvt_pk_bf16_f32 v203, v50, v51\n"
    "v_mfma_f32_16x16x32_bf16 v[16:19], a[64:67], v[192:195], 0\n"
    "v_add_f32 v80, v80, v81\n"
    "v_cmp_nge_f32 s[66:67], s61, v24\n"
    "v_mfma_f32_16x16x32_bf16 v[20:23], a[68:71], v[192:195], 0\n"
    "v_rcp_f32 v219, v80\n"
    "v_mfma_f32_16x16x32_bf16 v[0:3], a[72:75], v[196:199], v[0:3]\n"
    "v_mul_f32 v56, v219, v56\n"
    "v_mul_f32 v57, v219, v57\n"
    "v_mfma_f32_16x16x32_bf16 v[4:7], a[76:79], v[196:199], v[4:7]\n"
    "v_cvt_pk_bf16_f32 v144, v56, v57\n"
    "v_mul_f32 v58, v219, v58\n"
    "v_mfma_f32_16x16x32_bf16 v[8:11], a[80:83], v[196:199], v[8:11]\n"
    "v_mul_f32 v59, v219, v59\n"
    "v_cvt_pk_bf16_f32 v145, v58, v59\n"
    "v_mfma_f32_16x16x32_bf16 v[12:15], a[84:87], v[196:199], v[12:15]\n"
    "v_mul_f32 v60, v219, v60\n"
    "v_mul_f32 v61, v219, v61\n"
    "v_mfma_f32_16x16x32_bf16 v[16:19], a[88:91], v[196:199], v[16:19]\n"
    "v_cvt_pk_bf16_f32 v146, v60, v61\n"
    "v_mul_f32 v62, v219, v62\n"
    "v_mfma_f32_16x16x32_bf16 v[20:23], a[92:95], v[196:199], v[20:23]\n"
    "v_mul_f32 v63, v219, v63\n"
    "v_cvt_pk_bf16_f32 v147, v62, v63\n"
    "v_mfma_f32_16x16x32_bf16 v[0:3], a[144:147], v[200:203], v[0:3]\n"
    "v_mul_f32 v64, v219, v64\n"
    "v_mul_f32 v65, v219, v65\n"
    "v_mfma_f32_16x16x32_bf16 v[4:7], a[148:151], v[200:203], v[4:7]\n"
    "v_cvt_pk_bf16_f32 v148, v64, v65\n"
    "v_mul_f32 v66, v219, v66\n"
    "v_mfma_f32_16x16x32_bf16 v[8:11], a[152:155], v[200:203], v[8:11]\n"
    "v_mul_f32 v67, v219, v67\n"
    "v_cvt_pk_bf16_f32 v149, v66, v67\n"
    "v_mfma_f32_16x16x32_bf16 v[12:15], a[156:159], v[200:203], v[12:15]\n"
    "v_mul_f32 v68, v219, v68\n"
    "v_mul_f32 v69, v219, v69\n"
    "v_mfma_f32_16x16x32_bf16 v[16:19], a[160:163], v[200:203], v[16:19]\n"
    "v_cvt_pk_bf16_f32 v150, v68, v69\n"
    "v_mul_f32 v70, v219, v70\n"
    "v_mfma_f32_16x16x32_bf16 v[20:23], a[164:167], v[200:203], v[20:23]\n"
    "v_mul_f32 v71, v219, v71\n"
    "v_cvt_pk_bf16_f32 v151, v70, v71\n"
    "v_mfma_f32_16x16x32_bf16 v[28:31], a[48:51], v[144:147], 0\n"
    "v_mul_f32 v72, v219, v72\n"
    "v_mul_f32 v73, v219, v73\n"
    "v_mfma_f32_16x16x32_bf16 v[32:35], a[52:55], v[144:147], 0\n"
    "v_cvt_pk_bf16_f32 v160, v72, v73\n"
    "v_mul_f32 v74, v219, v74\n"
    "v_mfma_f32_16x16x32_bf16 v[36:39], a[56:59], v[144:147], 0\n"
    "v_mul_f32 v75, v219, v75\n"
    "v_cvt_pk_bf16_f32 v161, v74, v75\n"
    "v_mfma_f32_16x16x32_bf16 v[40:43], a[60:63], v[144:147], 0\n"
    "v_mul_f32 v76, v219, v76\n"
    "v_mul_f32 v77, v219, v77\n"
    "v_mfma_f32_16x16x32_bf16 v[44:47], a[64:67], v[144:147], 0\n"
    "v_cvt_pk_bf16_f32 v162, v76, v77\n"
    "v_mul_f32 v78, v219, v78\n"
    "v_mfma_f32_16x16x32_bf16 v[48:51], a[68:71], v[144:147], 0\n"
    "v_mul_f32 v79, v219, v79\n"
    "v_cvt_pk_bf16_f32 v163, v78, v79\n"
    "v_mfma_f32_16x16x32_bf16 v[28:31], a[72:75], v[148:151], v[28:31]\n"
    "v_cvt_pk_bf16_f32 v204, v0, v1\n"
    "v_cvt_pk_bf16_f32 v205, v2, v3\n"
    "v_mfma_f32_16x16x32_bf16 v[32:35], a[76:79], v[148:151], v[32:35]\n"
    "v_cvt_pk_bf16_f32 v206, v4, v5\n"
    "v_cvt_pk_bf16_f32 v207, v6, v7\n"
    "v_mfma_f32_16x16x32_bf16 v[36:39], a[80:83], v[148:151], v[36:39]\n"
    "global_store_dwordx4 v233, v[204:207], s[86:87] offset:0 sc1\n"
    "v_mfma_f32_16x16x32_bf16 v[40:43], a[84:87], v[148:151], v[40:43]\n"
    "v_cvt_pk_bf16_f32 v208, v8, v9\n"
    "v_cvt_pk_bf16_f32 v209, v10, v11\n"
    "v_mfma_f32_16x16x32_bf16 v[44:47], a[88:91], v[148:151], v[44:47]\n"
    "v_cvt_pk_bf16_f32 v210, v12, v13\n"
    "v_cvt_pk_bf16_f32 v211, v14, v15\n"
    "v_mfma_f32_16x16x32_bf16 v[48:51], a[92:95], v[148:151], v[48:51]\n"
    "global_store_dwordx4 v233, v[208:211], s[86:87] offset:64 sc1\n"
    "v_mfma_f32_16x16x32_bf16 v[28:31], a[144:147], v[160:163], v[28:31]\n"
    "v_cvt_pk_bf16_f32 v212, v16, v17\n"
    "v_cvt_pk_bf16_f32 v213, v18, v19\n"
    "v_mfma_f32_16x16x32_bf16 v[32:35], a[148:151], v[160:163], v[32:35]\n"
    "v_cvt_pk_bf16_f32 v214, v20, v21\n"
    "v_cvt_pk_bf16_f32 v215, v22, v23\n"
    "v_mfma_f32_16x16x32_bf16 v[36:39], a[152:155], v[160:163], v[36:39]\n"
    "global_store_dwordx4 v233, v[212:215], s[86:87] offset:128 sc1\n"
    "v_mfma_f32_16x16x32_bf16 v[40:43], a[156:159], v[160:163], v[40:43]\n"
    "v_cmp_nle_f32 s[68:69], s60, v52\n"
    "v_cmp_nge_f32 s[70:71], s61, v52\n"
    "v_mfma_f32_16x16x32_bf16 v[44:47], a[160:163], v[160:163], v[44:47]\n"
    "v_cmp_nle_f32 s[72:73], s60, v80\n"
    "v_cmp_nge_f32 s[74:75], s61, v80\n"
    "v_mfma_f32_16x16x32_bf16 v[48:51], a[164:167], v[160:163], v[48:51]\n"
    "v_cvt_pk_bf16_f32 v204, v28, v29\n"
    "v_cvt_pk_bf16_f32 v205, v30, v31\n"
    "s_nop 3\n"
    "v_cvt_pk_bf16_f32 v206, v32, v33\n"
    "v_cvt_pk_bf16_f32 v207, v34, v35\n"
    "global_store_dwordx4 v234, v[204:207], s[86:87] offset:0 sc1\n"
    "twins_p7:\n"
    "s_cbranch_execz twins_ret\n"
    "v_cvt_pk_bf16_f32 v208, v36, v37\n"
    "v_cvt_pk_bf16_f32 v209, v38, v39\n"
    "v_cvt_pk_bf16_f32 v210, v40, v41\n"
    "v_cvt_pk_bf16_f32 v211, v42, v43\n"
    "global_store_dwordx4 v234, v[208:211], s[86:87] offset:64 sc1\n"
    "v_cvt_pk_bf16_f32 v212, v44, v45\n"
    "v_cvt_pk_bf16_f32 v213, v46, v47\n"
    "v_cvt_pk_bf16_f32 v214, v48, v49\n"
    "v_cvt_pk_bf16_f32 v215, v50, v51\n"
    "global_store_dwordx4 v234, v[212:215], s[86:87] offset:128 sc1\n"
    "v_cmp_eq_u64 vcc, s[58:59], v[224:225]\n"
    "s_or_b64 vcc, vcc, s[64:65]\n"
    "s_or_b64 vcc, vcc, s[66:67]\n"
    "s_or_b64 vcc, vcc, s[68:69]\n"
    "s_or_b64 vcc, vcc, s[70:71]\n"
    "s_or_b64 vcc, vcc, s[72:73]\n"
    "s_or_b64 vcc, vcc, s[74:75]\n"
    "s_mov_b64 s[64:65], vcc\n"
    "s_cmp_lg_u32 s88, 3\n"
    "s_cbranch_scc1 END%=\n"
    "ds_read_b128 v[0:3], v236 offset:0\n"
    "ds_read_b128 v[4:7], v236 offset:1024\n"
    "ds_read_b32 v32, v237 offset:0\n"
    "ds_read_b128 v[8:11], v236 offset:7168\n"
    "ds_read_b128 v[12:15], v236 offset:8192\n"
    "ds_read_b32 v33, v237 offset:7168\n"
    "ds_read_b128 v[16:19], v236 offset:14336\n"
    "ds_read_b128 v[20:23], v236 offset:15360\n"
    "ds_read_b32 v34, v237 offset:14336\n"
    "ds_read_b128 v[24:27], v236 offset:21504\n"
    "ds_read_b128 v[28:31], v236 offset:22528\n"
    "ds_read_b32 v35, v237 offset:21504\n"
    "s_mov_b32 exec_hi, 0xffff\n"
    "s_waitcnt lgkmcnt(0)\n"
    "v_add_f32 v32, v32, v33\n"
    "v_add_f32 v34, v34, v35\n"
    "v_add_f32 v32, v32, v34\n"
    "v_rcp_f32 v33, v32\n"
    "v_cmp_nle_f32 vcc, s60, v32\n"
    "s_or_b64 s[64:65], s[64:65], vcc\n"
    "v_cmp_nge_f32 vcc, s61, v32\n"
    "s_or_b64 s[64:65], s[64:65], vcc\n"
    "v_pk_add_f32 v[0:1], v[0:1], v[8:9]\n"
    "v_pk_add_f32 v[16:17], v[16:17], v[24:25]\n"
    "v_pk_add_f32 v[2:3], v[2:3], v[10:11]\n"
    "v_pk_add_f32 v[18:19], v[18:19], v[26:27]\n"
    "v_pk_add_f32 v[4:5], v[4:5], v[12:13]\n"
    "v_pk_add_f32 v[20:21], v[20:21], v[28:29]\n"
    "v_pk_add_f32 v[6:7], v[6:7], v[14:15]\n"
    "v_pk_add_f32 v[22:23], v[22:23], v[30:31]\n"
    "v_mov_b32 v34, v33\n"
    "v_mov_b32 v35, v33\n"
    "v_pk_add_f32 v[0:1], v[0:1], v[16:17]\n"
    "v_pk_add_f32 v[2:3], v[2:3], v[18:19]\n"
    "v_pk_add_f32 v[4:5], v[4:5], v[20:21]\n"
    "v_pk_add_f32 v[6:7], v[6:7], v[22:23]\n"
    "v_pk_mul_f32 v[0:1], v[0:1], v[34:35]\n"
    "v_pk_mul_f32 v[2:3], v[2:3], v[34:35]\n"
    "v_pk_mul_f32 v[4:5], v[4:5], v[34:35]\n"
    "v_pk_mul_f32 v[6:7], v[6:7], v[34:35]\n"
    "v_cvt_pk_bf16_f32 v36, v0, v1\n"
    "v_cvt_pk_bf16_f32 v37, v2, v3\n"
    "v_cvt_pk_bf16_f32 v38, v4, v5\n"
    "v_cvt_pk_bf16_f32 v39, v6, v7\n"
    "global_store_dwordx4 v235, v[36:39], s[86:87] sc1\n"
    "s_movk_i32 s38, 0x207\n"
    "v_cmp_class_f32 vcc, v0, s38\n"
    "s_or_b64 s[64:65], s[64:65], vcc\n"
    "v_cmp_class_f32 vcc, v1, s38\n"
    "s_or_b64 s[64:65], s[64:65], vcc\n"
    "v_cmp_class_f32 vcc, v2, s38\n"
    "s_or_b64 s[64:65], s[64:65], vcc\n"
    "v_cmp_class_f32 vcc, v3, s38\n"
    "s_or_b64 s[64:65], s[64:65], vcc\n"
    "v_cmp_class_f32 vcc, v4, s38\n"
    "s_or_b64 s[64:65], s[64:65], vcc\n"
    "v_cmp_class_f32 vcc, v5, s38\n"
    "s_or_b64 s[64:65], s[64:65], vcc\n"
    "v_cmp_class_f32 vcc, v6, s38\n"
    "s_or_b64 s[64:65], s[64:65], vcc\n"
    "v_cmp_class_f32 vcc, v7, s38\n"
    "s_or_b64 s[64:65], s[64:65], vcc\n"
    "s_mov_b64 exec, -1\n"
    "END%=:\n"
    "s_cmp_lg_u32 s89, 0\n"
    "s_cbranch_scc1 NE%=\n"
    "v_mov_b32 v0, 212992\n"
    "s_mov_b64 exec, 1\n"
    "global_store_dwordx2 v0, v[240:241], s[84:85] sc1\n"
    "s_mov_b64 exec, -1\n"
    "NE%=:\n"
    "s_mov_b32 m0, s36\n"
    "s_cmp_lg_u64 s[64:65], 0\n"
    "s_cselect_b32 %[fail], 1, 0\n"
    "s_cbranch_scc0 FIN%=\n"
    "s_lshl_b32 s38, s40, 1\n"
    "s_nop 1\n"
    "v_add_u32 v0, s38, v216\n"
    "ds_write_b128 v0, a[0:3] offset:0\n"
    "ds_write_b128 v0, a[4:7] offset:1024\n"
    "ds_write_b128 v0, a[8:11] offset:2048\n"
    "ds_write_b128 v0, a[12:15] offset:3072\n"
    "ds_write_b128 v0, a[16:19] offset:4096\n"
    "ds_write_b128 v0, a[20:23] offset:5120\n"
    "ds_write_b128 v0, a[24:27] offset:6144\n"
    "ds_write_b128 v0, a[28:31] offset:7168\n"
    "ds_write_b128 v0, a[32:35] offset:8192\n"
    "ds_write_b128 v0, a[36:39] offset:9216\n"
    "ds_write_b128 v0, a[40:43] offset:10240\n"
    "ds_write_b128 v0, a[44:47] offset:11264\n"
    "s_waitcnt vmcnt(0) lgkmcnt(0)\n"
    "s_branch FIN%=\n"
    "DG0%=:\n"
    "global_store_dwordx2 v230, v[62:63], s[84:85] sc1\n"
    "s_waitcnt vmcnt(0)\n"
    "s_branch DB0%=\n"
    "Rc%=:\n"
    "s_cmp_eq_u32 s37, 0\n"
    "s_cbranch_scc1 Bc%=\n"
    "s_sub_u32 s37, s37, 1\n"
    "s_sleep 2\n"
    "global_load_dwordx2 v[224:225], v231, s[90:91] sc1\n"
    "s_waitcnt vmcnt(0)\n"
    "v_cmp_ne_u64 vcc, s[58:59], v[224:225]\n"
    "s_and_b32 s38, vcc_lo, 0xffff0000\n"
    "s_cbranch_scc1 Rc%=\n"
    "s_branch Bc%=\n"
    "Rd%=:\n"
    "s_cmp_eq_u32 s37, 0\n"
    "s_cbranch_scc1 Bd%=\n"
    "s_sub_u32 s37, s37, 1\n"
    "s_sleep 2\n"
    "global_load_dwordx2 v[224:225], v231, s[90:91] sc1\n"
    "s_waitcnt vmcnt(0)\n"
    "v_cmp_ne_u64 vcc, s[58:59], v[224:225]\n"
    "s_cmp_lg_u64 vcc, 0\n"
    "s_cbranch_scc1 Rd%=\n"
    "s_branch Bd%=\n"
    "Rv%=:\n"
    "s_cmp_eq_u32 s37, 0\n"
    "s_cbranch_scc1 Bv%=\n"
    "s_sub_u32 s37, s37, 1\n"
    "s_sleep 2\n"
    "global_load_dwordx2 v[224:225], v231, s[92:93] sc1\n"
    "s_waitcnt vmcnt(0)\n"
    "v_cmp_ne_u64 vcc, s[58:59], v[224:225]\n"
    "s_and_b32 s38, vcc_hi, 0x3ffff\n"
    "s_cbranch_scc1 Rv%=\n"
    "s_branch Bv%=\n"
    "FIN%=:\n"
    "twins_end:\n"
    : [fail] "=s"(fail), "+{v[216:231]}"(va), "+{v[232:247]}"(vb), "+{v[248:251]}"(vc), "+{s75}"(junk)
    : "{s[84:99]}"(sa), "{s[28:35]}"(sb)
    : "memory", "scc", "vcc", "v0","v1","v2","v3","v4","v5","v6","v7","v8","v9","v10","v11","v12","v13","v14","v15","v16","v17","v18","v19","v20","v21","v22","v23","v24","v25","v26","v27","v28","v29","v30","v31","v32","v33","v34","v35","v36","v37","v38","v39","v40","v41","v42","v43","v44","v45","v46","v47","v48","v49","v50","v51","v52","v53","v54","v55","v56","v57","v58","v59","v60","v61","v62","v63","v64","v65","v66","v67","v68","v69","v70","v71","v72","v73","v74","v75","v76","v77","v78","v79","v80","v81","v82","v83","v84","v85","v86","v87","v88","v89","v90","v91","v92","v93","v94","v95","v96","v97","v98","v99","v100","v101","v102","v103","v104","v105","v106","v107","v108","v109","v110","v111","v112","v113","v114","v115","v116","v117","v118","v119","v120","v121","v122","v123","v124","v125","v126","v127","v128","v129","v130","v131","v132","v133","v134","v135","v136","v137","v138","v139","v140","v141","v142","v143","v144","v145","v146","v147","v148","v149","v150","v151","v152","v153","v154","v155","v156","v157","v158","v159","v160","v161","v162","v163","v164","v165","v166","v167","v168","v169","v170","v171","v172","v173","v174","v175","v176","v177","v178","v179","v180","v181","v182","v183","v184","v185","v186","v187","v188","v189","v190","v191","v192","v193","v194","v195","v196","v197","v198","v199","v200","v201","v202","v203","v204","v205","v206","v207","v208","v209","v210","v211","v212","v213","v214","v215","a0","a1","a2","a3","a4","a5","a6","a7","a8","a9","a10","a11","a12","a13","a14","a15","a16","a17","a18","a19","a20","a21","a22","a23","a24","a25","a26","a27","a28","a29","a30","a31","a32","a33","a34","a35","a36","a37","a38","a39","a40","a41","a42","a43","a44","a45","a46","a47","a48","a49","a50","a51","a52","a53","a54","a55","a56","a57","a58","a59","a60","a61","a62","a63","a64","a65","a66","a67","a68","a69","a70","a71","a72","a73","a74","a75","a76","a77","a78","a79","a80","a81","a82","a83","a84","a85","a86","a87","a88","a89","a90","a91","a92","a93","a94","a95","a96","a97","a98","a99","a100","a101","a102","a103","a104","a105","a106","a107","a108","a109","a110","a111","a112","a113","a114","a115","a116","a117","a118","a119","a120","a121","a122","a123","a124","a125","a126","a127","a128","a129","a130","a131","a132","a133","a134","a135","a136","a137","a138","a139","a140","a141","a142","a143","a144","a145","a146","a147","a148","a149","a150","a151","a152","a153","a154","a155","a156","a157","a158","a159","a160","a161","a162","a163","a164","a165","a166","a167","a168","a169","a170","a171","a172","a173","a174","a175","a176","a177","a178","a179","a180","a181","a182","a183","a184","a185","a186","a187","a188","a189","a190","a191","a192","a193","a194","a195","a196","a197","a198","a199","a200","a201","a202","a203","a204","a205","a206","a207","a208","a209","a210","a211","a212","a213","a214","a215","a216","a217","a218","a219","a220","a221","a222","a223","a224","a225","a226","a227","a228","a229","a230","a231","a232","a233","a234","a235","a236","a237","a238","a239","a240","a241","a242","a243","s36","s37","s38","s39","s40","s41","s42","s43","s44","s45","s46","s47","s48","s49","s50","s51","s52","s53","s54","s55","s56","s57","s58","s59","s60","s61","s62","s63","s64","s65","s66","s67","s68","s69","s70","s71","s72","s73","s74","s76","s77","s78","s79","s80","s81","s82","s83");
  if (__builtin_expect(fail, 0)) {
    const int ln = va[0] >> 4;
    for (int b = 0; b < (wid == 3 ? 4 : 3); ++b) {
      const unsigned oo = token_off(b < 3 ? 48 * wid + 16 * b + (ln & 15) : 192 + (ln & 3)) + (ln >> 4) * 16;
      const int j16 = ln & 15, gg = ln >> 4;
      slow_block(wid * 12288 + b * 3072 + va[0], 49152 + wid * 6144, j16 * 192 + ((gg ^ ((-(j16 >> 2)) & 3)) << 4),
                 (4 * gg + (j16 >> 2)) * 192 + ((((j16 & 3) >> 1) ^ ((-gg) & 3)) << 4) + (j16 & 1) * 8, ws, outb + oo, b < 3 || (ln & 15) < 4);
    }
  }
}

extern "C" void kernel_launch(void* const* d_in, const int* in_sizes, int n_in, void* d_out, int out_size, void* d_ws, size_t ws_size, hipStream_t stream) {
  twins_attn<<<256, 256, 0, stream>>>((const char*)d_in[0], (const char*)d_in[1], (const char*)d_in[2], (const char*)d_in[3], (char*)d_out, (char*)d_ws);
}
```
